```python
import torch

def _softmax(x, axis=-1):
    return torch.softmax(x, dim=axis)
def _gelu(x):
    return torch.nn.functional.gelu(x, approximate="tanh")
def _silu(x):
    return torch.nn.functional.silu(x)
def _sigmoid(x):
    return torch.sigmoid(x)
def _layernorm(x, axis=-1, eps=1e-5):
    m = x.mean(dim=axis, keepdim=True)
    v = x.var(dim=axis, keepdim=True, unbiased=False)
    return (x - m) / torch.sqrt(v + eps)
def _scatter_add(tgt, idx, src, axis=0):
    return tgt.clone().index_add_(axis, idx, src)
def _segment_sum(src, seg, n):
    out = torch.zeros((n,) + src.shape[1:], dtype=src.dtype)
    return out.index_add_(0, seg, src)
def _pad(x, pw):
    flat = tuple(v for pair in reversed(pw) for v in pair)
    return torch.nn.functional.pad(x, flat)

kernel_name = 'standalone__sliding_block_cached__decode'

def setup_inputs():

    _w = (lambda a: (lambda t: t.to(torch.bfloat16) if t.is_floating_point() else t)(torch.as_tensor(a)))
    B, Sq, D = 1, 1, 512
    Hq, Hkv, Sk, Dh = 32, 8, 32768, 128
    assert Hq % Hkv == 0, (Hq, Hkv)
    return {
        "x": _w((lambda *s: torch.randn(*s, dtype=torch.bfloat16) * 0.8)(B, Sq, D)),
        "k_cache": _w((lambda *s: torch.randn(*s, dtype=torch.bfloat16) * 0.8)(B, Hkv, Sk, Dh)),
        "v_cache": _w((lambda *s: torch.randn(*s, dtype=torch.bfloat16) * 0.05)(B, Hkv, Sk, Dh)),
        "wq": _w((lambda *s: torch.randn(*s, dtype=torch.bfloat16) * 0.8)(D, Hq * Dh) * (D ** -0.5)),
        "wo": _w((lambda *s: torch.randn(*s, dtype=torch.bfloat16) * 0.05)(Hq * Dh, D) * ((Hq * Dh) ** -0.5)),
    }

def reference(x, k_cache, v_cache, wq, wo):
    B, Sq, D = x.shape
    Hq, Dh = 32, 128
    Hkv, Sk = k_cache.shape[1], k_cache.shape[2]
    W = min(4096, max(256, Sk // 4))
    rep = Hq // Hkv
    q = (lambda x, *d: x.permute(*d))((x @ wq).reshape(B, Sq, Hq, Dh), 0, 2, 1, 3)
    k = (lambda x, n, axis: torch.repeat_interleave(x, n, dim=axis))(k_cache[:, :, Sk - W:, :], rep, axis=1)
    v = (lambda x, n, axis: torch.repeat_interleave(x, n, dim=axis))(v_cache[:, :, Sk - W:, :], rep, axis=1)
    scores = torch.einsum("bhqd,bhkd->bhqk", q, k) * (Dh ** -0.5)
    p = _softmax(scores, axis=-1)
    o = torch.einsum("bhqk,bhkd->bhqd", p, v)
    return (lambda x, *d: x.permute(*d))(o, 0, 2, 1, 3).reshape(B, Sq, Hq * Dh) @ wo
```

```cpp
#include <hip/hip_runtime.h>
#include <hip/hip_bf16.h>
#include <stdint.h>

typedef unsigned short u16;
__device__ __forceinline__ float bf2f(u16 v) { return __uint_as_float(((unsigned)v) << 16); }
__device__ __forceinline__ u16 f2bf(float f) { unsigned u = __float_as_uint(f); return (u16)((u + 0x7fffu + ((u >> 16) & 1u)) >> 16); }

constexpr int D = 512, HQ = 32, HKV = 8, SK = 32768, DH = 128, W = 4096, NS = 32, KPS = W / NS;

__global__ void k_q(const u16* x, const u16* wq, u16* q) {
  int n = blockIdx.x * 64 + threadIdx.x;
  float a = 0.f;
  for (int d = 0; d < D; ++d) a += bf2f(x[d]) * bf2f(wq[(size_t)d * 4096 + n]);
  q[n] = f2bf(a);
}
__global__ void k_attn(const u16* q, const u16* kc, const u16* vc, float* pm, float* pl, float* po) {
  int s = blockIdx.x, g = blockIdx.y, t = threadIdx.x;
  __shared__ float sp[4][128];
  __shared__ float red[4][128];
  const u16* krow = kc + ((size_t)g * SK + (SK - W) + s * KPS + t) * DH;
  float sc[4];
  for (int h = 0; h < 4; ++h) {
    float a = 0.f;
    for (int c = 0; c < DH; ++c) a += bf2f(q[(g * 4 + h) * DH + c]) * bf2f(krow[c]);
    float sb = bf2f(f2bf(a));
    sc[h] = bf2f(f2bf(sb * 0.08838834764831845f));
    red[h][t] = sc[h];
  }
  __syncthreads();
  for (int h = 0; h < 4; ++h) {
    float m = -INFINITY;
    for (int j = 0; j < 128; ++j) m = fmaxf(m, red[h][j]);
    sp[h][t] = __expf(sc[h] - m);
    if (t == 0) pm[(g * 4 + h) * NS + s] = m;
  }
  __syncthreads();
  for (int h = 0; h < 4; ++h) {
    float l = 0.f, o = 0.f;
    for (int j = 0; j < 128; ++j) {
      l += sp[h][j];
      o += sp[h][j] * bf2f(vc[((size_t)g * SK + (SK - W) + s * KPS + j) * DH + t]);
    }
    po[((size_t)(g * 4 + h) * NS + s) * DH + t] = o;
    if (t == 0) pl[(g * 4 + h) * NS + s] = l;
  }
}
__global__ void k_comb(const float* pm, const float* pl, const float* po, u16* o) {
  int h = blockIdx.x, t = threadIdx.x;
  float M = -INFINITY;
  for (int s = 0; s < NS; ++s) M = fmaxf(M, pm[h * NS + s]);
  float L = 0.f, a = 0.f;
  for (int s = 0; s < NS; ++s) {
    float w = __expf(pm[h * NS + s] - M);
    L += w * pl[h * NS + s];
    a += w * po[((size_t)h * NS + s) * DH + t];
  }
  o[h * DH + t] = f2bf(a / L);
}
__global__ void k_out(const u16* o, const u16* wo, u16* out) {
  int n = blockIdx.x * 64 + threadIdx.x;
  float a = 0.f;
  for (int k = 0; k < 4096; ++k) a += bf2f(o[k]) * bf2f(wo[(size_t)k * D + n]);
  out[n] = f2bf(a);
}

extern "C" void kernel_launch(void* const* d_in, const int* in_sizes, int n_in, void* d_out, int out_size, void* d_ws, size_t ws_size, hipStream_t stream) {
  const u16* x = (const u16*)d_in[0];
  const u16* kc = (const u16*)d_in[1];
  const u16* vc = (const u16*)d_in[2];
  const u16* wq = (const u16*)d_in[3];
  const u16* wo = (const u16*)d_in[4];
  char* ws = (char*)d_ws;
  u16* q = (u16*)ws;
  u16* o = (u16*)(ws + 65536);
  float* pm = (float*)(ws + 131072);
  float* pl = (float*)(ws + 196608);
  float* po = (float*)(ws + 262144);
  k_q<<<64, 64, 0, stream>>>(x, wq, q);
  k_attn<<<dim3(NS, HKV), 128, 0, stream>>>(q, kc, vc, pm, pl, po);
  k_comb<<<32, 128, 0, stream>>>(pm, pl, po, o);
  k_out<<<8, 64, 0, stream>>>(o, wo, (u16*)d_out);
}
```

```cpp
#include <hip/hip_runtime.h>
#include <hip/hip_bf16.h>
#include <stdint.h>

typedef unsigned short u16;
typedef unsigned long long u64;
typedef __attribute__((ext_vector_type(8))) __bf16 bf16x8;
typedef __attribute__((ext_vector_type(4))) float f32x4;
typedef __attribute__((ext_vector_type(4))) unsigned u32x4;
#define GAS __attribute__((address_space(1)))
typedef GAS u64 gu64;
#define RLX_AGENT __ATOMIC_RELAXED, __HIP_MEMORY_SCOPE_AGENT

constexpr int D = 512, HQ = 32, HKV = 8, SK = 32768, DH = 128, WIN = 4096;
constexpr size_t WS_E = 0;
constexpr size_t WS_H1 = 4096;
constexpr size_t WS_H2ML = WS_H1 + 32768;
constexpr size_t WS_H2O = 65536;
constexpr size_t WS_H3 = WS_H2O + 524288;
constexpr float LOG2E = 1.4426950408889634f;
constexpr float SCALE = 0.08838834764831845f;
constexpr unsigned SPIN_CAP = 1u << 22;

__device__ __forceinline__ float bf2f(unsigned v) { return __uint_as_float(v << 16); }
__device__ __forceinline__ unsigned f2bf(float f) { __bf16 b = (__bf16)f; return (unsigned)__builtin_bit_cast(unsigned short, b); }
__device__ __forceinline__ float rbf(float f) { return bf2f(f2bf(f)); }
__device__ __forceinline__ u32x4 ldg16(const u16* p) { return __builtin_nontemporal_load(reinterpret_cast<const u32x4*>(p)); }
__device__ __forceinline__ float lo16(unsigned v) { return __uint_as_float(v << 16); }
__device__ __forceinline__ float hi16(unsigned v) { return __uint_as_float(v & 0xffff0000u); }
#define CBAR() do { asm volatile("" ::: "memory"); __builtin_amdgcn_sched_barrier(0); } while (0)

__global__ __launch_bounds__(256, 1) void fused_decode(const u16* __restrict__ x, const u16* __restrict__ kc, const u16* __restrict__ vc,
                                                       const u16* __restrict__ wq, const u16* __restrict__ wo, u16* __restrict__ out, char* ws) {
  __shared__ __attribute__((aligned(16))) float s_qred[4][16];
  __shared__ __attribute__((aligned(16))) unsigned s_q[256];
  __shared__ __attribute__((aligned(16))) float s_om[4][4][128];
  __shared__ __attribute__((aligned(16))) float s_mm[4][4], s_ll[4][4];
  __shared__ __attribute__((aligned(16))) float s_cred[4][64];

  const int b = blockIdx.x, g = b & 7, j = b >> 3;
  const int t = threadIdx.x, lane = t & 63, w = __builtin_amdgcn_readfirstlane(t >> 6);
  gu64* pE = (gu64*)(ws + WS_E);
  gu64* H1 = (gu64*)(ws + WS_H1);
  gu64* H2ML = (gu64*)(ws + WS_H2ML);
  gu64* H2O = (gu64*)(ws + WS_H2O);
  gu64* H3 = (gu64*)(ws + WS_H3);

  const u64 E = __hip_atomic_load(pE, RLX_AGENT);
  gu64* myO = H2O + ((size_t)((4 * g + w) * 32 + j)) * 64 + lane;
  gu64* myML = H2ML + (4 * g + w) * 32 + j;
  __hip_atomic_store(myO, 0ull, RLX_AGENT);
  if (lane == 0) __hip_atomic_store(myML, 0ull, RLX_AGENT);
  CBAR();
  const int n0 = g * 512 + j * 16;
  const int half = t & 1, dbase = t >> 1;
  unsigned xv[4];
  u32x4 wqv[4];
#pragma unroll
  for (int i = 0; i < 4; ++i) xv[i] = x[dbase + 128 * i];
#pragma unroll
  for (int i = 0; i < 4; ++i) wqv[i] = ldg16(wq + (size_t)(dbase + 128 * i) * 4096 + n0 + 8 * half);
  CBAR();
  const int kk = lane >> 4, c16 = lane & 15;
  const size_t key0 = (size_t)g * SK + (SK - WIN) + j * 128 + w * 32;
  u32x4 kf[2][4], vr[8];
#pragma unroll
  for (int grp = 0; grp < 2; ++grp)
#pragma unroll
    for (int st = 0; st < 4; ++st) kf[grp][st] = ldg16(kc + (key0 + 16 * grp + c16) * DH + 32 * st + 8 * kk);
#pragma unroll
  for (int i = 0; i < 8; ++i) vr[i] = ldg16(vc + (key0 + 16 * (i >> 2) + 4 * kk + (i & 3)) * DH + 8 * c16);
  CBAR();

  {
    float acc[8];
#pragma unroll
    for (int k = 0; k < 8; ++k) acc[k] = 0.f;
#pragma unroll
    for (int i = 0; i < 4; ++i) {
      const float xf = bf2f(xv[i]);
#pragma unroll
      for (int k = 0; k < 4; ++k) {
        acc[2 * k] = fmaf(xf, lo16(wqv[i][k]), acc[2 * k]);
        acc[2 * k + 1] = fmaf(xf, hi16(wqv[i][k]), acc[2 * k + 1]);
      }
    }
    float a4[4], a2[2], a1;
    const bool u1 = lane & 2, u2 = lane & 4, u3 = lane & 8;
#pragma unroll
    for (int k = 0; k < 4; ++k) { const float snd = u1 ? acc[k] : acc[k + 4], kp = u1 ? acc[k + 4] : acc[k]; a4[k] = kp + __shfl_xor(snd, 2); }
#pragma unroll
    for (int k = 0; k < 2; ++k) { const float snd = u2 ? a4[k] : a4[k + 2], kp = u2 ? a4[k + 2] : a4[k]; a2[k] = kp + __shfl_xor(snd, 4); }
    { const float snd = u3 ? a2[0] : a2[1], kp = u3 ? a2[1] : a2[0]; a1 = kp + __shfl_xor(snd, 8); }
    a1 += __shfl_xor(a1, 16);
    a1 += __shfl_xor(a1, 32);
    const int col = 8 * (lane & 1) + 4 * ((lane >> 1) & 1) + 2 * ((lane >> 2) & 1) + ((lane >> 3) & 1);
    if (lane < 16) s_qred[w][col] = a1;
  }
  __syncthreads();
  const u64 tag48 = (E + 1) & 0xFFFFFFFFFFFFull;
  const u64 tag40 = (E + 1) & 0xFFFFFFFFFFull;
  if (t < 16) {
    const float qv = (s_qred[0][t] + s_qred[1][t]) + (s_qred[2][t] + s_qred[3][t]);
    __hip_atomic_store(H1 + n0 + t, ((u64)f2bf(qv) << 48) | tag48, RLX_AGENT);
  }
  {
    gu64* src = H1 + g * 512 + 2 * t;
    u64 a0, a1;
    for (unsigned sp = 0; sp < SPIN_CAP; ++sp) {
      a0 = __hip_atomic_load(src, RLX_AGENT);
      a1 = __hip_atomic_load(src + 1, RLX_AGENT);
      const bool ok = ((a0 & 0xFFFFFFFFFFFFull) == tag48) && ((a1 & 0xFFFFFFFFFFFFull) == tag48);
      if (__all(ok)) break;
      __builtin_amdgcn_s_sleep(1);
    }
    s_q[t] = (unsigned)(a0 >> 48) | ((unsigned)(a1 >> 48) << 16);
  }
  CBAR();
  const int hh = j >> 3, h = 4 * g + hh, cg = j & 7;
  const int rg = t >> 3, c8 = t & 7;
  u32x4 wov[4];
#pragma unroll
  for (int i = 0; i < 4; ++i) wov[i] = ldg16(wo + (size_t)(h * 128 + 4 * rg + i) * D + cg * 64 + 8 * c8);
  CBAR();
  __syncthreads();

  {
    u32x4 qf[4];
#pragma unroll
    for (int st = 0; st < 4; ++st) {
      qf[st] = u32x4{0u, 0u, 0u, 0u};
      if (c16 < 4) qf[st] = *reinterpret_cast<const u32x4*>(&s_q[c16 * 64 + 16 * st + 4 * kk]);
    }
    f32x4 sacc[2];
#pragma unroll
    for (int grp = 0; grp < 2; ++grp) {
      sacc[grp] = f32x4{0.f, 0.f, 0.f, 0.f};
#pragma unroll
      for (int st = 0; st < 4; ++st)
        sacc[grp] = __builtin_amdgcn_mfma_f32_16x16x32_bf16(__builtin_bit_cast(bf16x8, kf[grp][st]), __builtin_bit_cast(bf16x8, qf[st]), sacc[grp], 0, 0, 0);
    }
    float sc[8];
#pragma unroll
    for (int i = 0; i < 8; ++i) sc[i] = rbf(rbf(sacc[i >> 2][i & 3]) * SCALE);
    float m = sc[0];
#pragma unroll
    for (int i = 1; i < 8; ++i) m = fmaxf(m, sc[i]);
    m = fmaxf(m, __shfl_xor(m, 16));
    m = fmaxf(m, __shfl_xor(m, 32));
    float p[8], l = 0.f;
#pragma unroll
    for (int i = 0; i < 8; ++i) { p[i] = __builtin_amdgcn_exp2f((sc[i] - m) * LOG2E); l += p[i]; }
    l += __shfl_xor(l, 16);
    l += __shfl_xor(l, 32);
    u32x4 pf;
#pragma unroll
    for (int d2 = 0; d2 < 4; ++d2) pf[d2] = f2bf(p[2 * d2]) | (f2bf(p[2 * d2 + 1]) << 16);
    f32x4 oacc[8];
#pragma unroll
    for (int jj = 0; jj < 8; ++jj) {
      u32x4 bfrag;
#pragma unroll
      for (int d2 = 0; d2 < 4; ++d2) {
        const unsigned lo = vr[2 * d2][jj >> 1], hi = vr[2 * d2 + 1][jj >> 1];
        bfrag[d2] = (jj & 1) ? __builtin_amdgcn_perm(hi, lo, 0x07060302u) : __builtin_amdgcn_perm(hi, lo, 0x05040100u);
      }
      oacc[jj] = __builtin_amdgcn_mfma_f32_16x16x32_bf16(__builtin_bit_cast(bf16x8, pf), __builtin_bit_cast(bf16x8, bfrag), f32x4{0.f, 0.f, 0.f, 0.f}, 0, 0, 0);
    }
    if (lane < 16) {
#pragma unroll
      for (int v = 0; v < 4; ++v) {
        *reinterpret_cast<f32x4*>(&s_om[w][v][8 * lane]) = f32x4{oacc[0][v], oacc[1][v], oacc[2][v], oacc[3][v]};
        *reinterpret_cast<f32x4*>(&s_om[w][v][8 * lane + 4]) = f32x4{oacc[4][v], oacc[5][v], oacc[6][v], oacc[7][v]};
      }
      if (lane < 4) { s_mm[w][lane] = m; s_ll[w][lane] = l; }
    }
  }
  __syncthreads();
  {
    float mw[4], M = -INFINITY;
#pragma unroll
    for (int i = 0; i < 4; ++i) { mw[i] = s_mm[i][w]; M = fmaxf(M, mw[i]); }
    float L = 0.f, o0 = 0.f, o1 = 0.f;
#pragma unroll
    for (int i = 0; i < 4; ++i) {
      const float e = __builtin_amdgcn_exp2f((mw[i] - M) * LOG2E);
      L = fmaf(e, s_ll[i][w], L);
      const float2 ov = *reinterpret_cast<const float2*>(&s_om[i][w][2 * lane]);
      o0 = fmaf(e, ov.x, o0);
      o1 = fmaf(e, ov.y, o1);
    }
    __hip_atomic_store(myO, (u64)__float_as_uint(o0) | ((u64)(__float_as_uint(o1) | 1u) << 32), RLX_AGENT);
    if (lane == 0) __hip_atomic_store(myML, (u64)__float_as_uint(M) | ((u64)(__float_as_uint(L) | 1u) << 32), RLX_AGENT);
  }
  {
    gu64* srcO = H2O + (size_t)(h * 32 + c8) * 64 + 2 * rg;
    gu64* srcML = H2ML + h * 32 + (lane & 31);
    u64 a[4][2], ml;
    for (unsigned sp = 0; sp < SPIN_CAP; ++sp) {
      bool ok = true;
#pragma unroll
      for (int i = 0; i < 4; ++i) {
        a[i][0] = __hip_atomic_load(srcO + (size_t)i * 8 * 64, RLX_AGENT);
        a[i][1] = __hip_atomic_load(srcO + (size_t)i * 8 * 64 + 1, RLX_AGENT);
      }
      ml = __hip_atomic_load(srcML, RLX_AGENT);
#pragma unroll
      for (int i = 0; i < 4; ++i) ok = ok && ((a[i][0] >> 32) & 1) && ((a[i][1] >> 32) & 1);
      ok = ok && ((ml >> 32) & 1);
      if (__all(ok)) break;
      __builtin_amdgcn_s_sleep(1);
    }
    const float ms = __uint_as_float((unsigned)ml), ls = __uint_as_float((unsigned)(ml >> 32));
    float M = ms;
#pragma unroll
    for (int o = 1; o < 32; o <<= 1) M = fmaxf(M, __shfl_xor(M, o));
    const float wsx = __builtin_amdgcn_exp2f((ms - M) * LOG2E);
    float L = wsx * ls;
#pragma unroll
    for (int o = 1; o < 32; o <<= 1) L += __shfl_xor(L, o);
    float o4[4] = {0.f, 0.f, 0.f, 0.f};
#pragma unroll
    for (int i = 0; i < 4; ++i) {
      const float wi = __shfl(wsx, c8 + 8 * i);
      o4[0] = fmaf(wi, __uint_as_float((unsigned)a[i][0]), o4[0]);
      o4[1] = fmaf(wi, __uint_as_float((unsigned)(a[i][0] >> 32)), o4[1]);
      o4[2] = fmaf(wi, __uint_as_float((unsigned)a[i][1]), o4[2]);
      o4[3] = fmaf(wi, __uint_as_float((unsigned)(a[i][1] >> 32)), o4[3]);
    }
#pragma unroll
    for (int o = 1; o < 8; o <<= 1)
#pragma unroll
      for (int k = 0; k < 4; ++k) o4[k] += __shfl_xor(o4[k], o);
    const float inv = 1.f / L;
    float acc[8];
#pragma unroll
    for (int k = 0; k < 8; ++k) acc[k] = 0.f;
#pragma unroll
    for (int i = 0; i < 4; ++i) {
      const float of = rbf(o4[i] * inv);
#pragma unroll
      for (int k = 0; k < 4; ++k) {
        acc[2 * k] = fmaf(of, lo16(wov[i][k]), acc[2 * k]);
        acc[2 * k + 1] = fmaf(of, hi16(wov[i][k]), acc[2 * k + 1]);
      }
    }
    float a4[4], a2[2], a1;
    const bool u1 = lane & 8, u2 = lane & 16, u3 = lane & 32;
#pragma unroll
    for (int k = 0; k < 4; ++k) { const float snd = u1 ? acc[k] : acc[k + 4], kp = u1 ? acc[k + 4] : acc[k]; a4[k] = kp + __shfl_xor(snd, 8); }
#pragma unroll
    for (int k = 0; k < 2; ++k) { const float snd = u2 ? a4[k] : a4[k + 2], kp = u2 ? a4[k + 2] : a4[k]; a2[k] = kp + __shfl_xor(snd, 16); }
    { const float snd = u3 ? a2[0] : a2[1], kp = u3 ? a2[1] : a2[0]; a1 = kp + __shfl_xor(snd, 32); }
    const int col = 8 * c8 + 4 * ((lane >> 3) & 1) + 2 * ((lane >> 4) & 1) + ((lane >> 5) & 1);
    s_cred[w][col] = a1;
  }
  __syncthreads();
  if (w != 0) return;
  {
    const float v = (s_cred[0][lane] + s_cred[1][lane]) + (s_cred[2][lane] + s_cred[3][lane]);
    const unsigned fb = (__float_as_uint(v) + 0x80u) >> 8;
    __hip_atomic_store(H3 + (size_t)h * 512 + cg * 64 + lane, ((u64)fb << 40) | tag40, RLX_AGENT);
  }
  {
    gu64* src = H3 + (size_t)(lane & 31) * 512 + 2 * b;
    u64 a0, a1;
    for (unsigned sp = 0; sp < SPIN_CAP; ++sp) {
      a0 = __hip_atomic_load(src, RLX_AGENT);
      a1 = __hip_atomic_load(src + 1, RLX_AGENT);
      const bool ok = ((a0 & 0xFFFFFFFFFFull) == tag40) && ((a1 & 0xFFFFFFFFFFull) == tag40);
      if (__all(ok)) break;
      __builtin_amdgcn_s_sleep(1);
    }
    float v0 = __uint_as_float((unsigned)(a0 >> 40) << 8), v1 = __uint_as_float((unsigned)(a1 >> 40) << 8);
#pragma unroll
    for (int o = 1; o < 32; o <<= 1) { v0 += __shfl_xor(v0, o); v1 += __shfl_xor(v1, o); }
    if (lane == 0) {
      *reinterpret_cast<unsigned*>(out + 2 * b) = f2bf(v0) | (f2bf(v1) << 16);
      if (b == 0) __hip_atomic_store(pE, E + 1, RLX_AGENT);
    }
  }
}

extern "C" void kernel_launch(void* const* d_in, const int* in_sizes, int n_in, void* d_out, int out_size, void* d_ws, size_t ws_size, hipStream_t stream) {
  fused_decode<<<256, 256, 0, stream>>>((const u16*)d_in[0], (const u16*)d_in[1], (const u16*)d_in[2], (const u16*)d_in[3], (const u16*)d_in[4],
                                        (u16*)d_out, (char*)d_ws);
}
```

```cpp
#include <hip/hip_runtime.h>
#include <hip/hip_bf16.h>
#include <stdint.h>

typedef unsigned short u16;
typedef unsigned long long u64;
typedef __attribute__((ext_vector_type(8))) __bf16 bf16x8;
typedef __attribute__((ext_vector_type(4))) float f32x4;
typedef __attribute__((ext_vector_type(4))) unsigned u32x4;
#define GAS __attribute__((address_space(1)))
typedef GAS u64 gu64;
#define RLX_AGENT __ATOMIC_RELAXED, __HIP_MEMORY_SCOPE_AGENT

constexpr int D = 512, HQ = 32, HKV = 8, SK = 32768, DH = 128, WIN = 4096;
constexpr unsigned WS_E = 0;
constexpr unsigned WS_H1 = 4096;
constexpr unsigned WS_H2ML = WS_H1 + 32768;
constexpr unsigned WS_H2O = 65536;
constexpr unsigned WS_H3 = WS_H2O + 524288;
constexpr unsigned WS_END = WS_H3 + 524288;
constexpr float LOG2E = 1.4426950408889634f;
constexpr float SCALE = 0.08838834764831845f;
constexpr unsigned SPIN_CAP = 1u << 22;

__device__ __forceinline__ float bf2f(unsigned v) { return __uint_as_float(v << 16); }
__device__ __forceinline__ unsigned f2bf(float f) { __bf16 b = (__bf16)f; return (unsigned)__builtin_bit_cast(unsigned short, b); }
__device__ __forceinline__ float rbf(float f) { return bf2f(f2bf(f)); }
__device__ __forceinline__ u32x4 ldg16(const u16* p) { return __builtin_nontemporal_load(reinterpret_cast<const u32x4*>(p)); }
__device__ __forceinline__ float lo16(unsigned v) { return __uint_as_float(v << 16); }
__device__ __forceinline__ float hi16(unsigned v) { return __uint_as_float(v & 0xffff0000u); }
template <int CTRL> __device__ __forceinline__ float dpp(float x) {
  return __builtin_bit_cast(float, __builtin_amdgcn_mov_dpp(__builtin_bit_cast(int, x), CTRL, 0xf, 0xf, true));
}
constexpr int XOR1 = 0xB1, XOR2 = 0x4E, ROR4 = 0x124, ROR8 = 0x128;
__device__ __forceinline__ float x16_sum(float x) { auto s = __builtin_amdgcn_permlane16_swap(__float_as_uint(x), __float_as_uint(x), false, false); return __uint_as_float(s[0]) + __uint_as_float(s[1]); }
__device__ __forceinline__ float x32_sum(float x) { auto s = __builtin_amdgcn_permlane32_swap(__float_as_uint(x), __float_as_uint(x), false, false); return __uint_as_float(s[0]) + __uint_as_float(s[1]); }
__device__ __forceinline__ float x16_max(float x) { auto s = __builtin_amdgcn_permlane16_swap(__float_as_uint(x), __float_as_uint(x), false, false); return fmaxf(__uint_as_float(s[0]), __uint_as_float(s[1])); }
__device__ __forceinline__ float x32_max(float x) { auto s = __builtin_amdgcn_permlane32_swap(__float_as_uint(x), __float_as_uint(x), false, false); return fmaxf(__uint_as_float(s[0]), __uint_as_float(s[1])); }
__device__ __forceinline__ float sum_b1to5(float x) { x += dpp<XOR2>(x); x += dpp<ROR4>(x); x += dpp<ROR8>(x); x = x16_sum(x); return x32_sum(x); }
__device__ __forceinline__ float max_b1to5(float x) { x = fmaxf(x, dpp<XOR2>(x)); x = fmaxf(x, dpp<ROR4>(x)); x = fmaxf(x, dpp<ROR8>(x)); x = x16_max(x); return x32_max(x); }
__device__ __forceinline__ float sum_b2to5(float x) { x += dpp<ROR4>(x); x += dpp<ROR8>(x); x = x16_sum(x); return x32_sum(x); }

#define STAMP_DECL
#define STAMP(i) do {} while (0)
#define STAMP_FLUSH(lo, hi) do {} while (0)
#define CBAR() do { asm volatile("" ::: "memory"); __builtin_amdgcn_sched_barrier(0); } while (0)
#define MEMBAR() asm volatile("" ::: "memory")

__global__ __launch_bounds__(512, 1) void fused_decode(const u16* __restrict__ x, const u16* __restrict__ kc, const u16* __restrict__ vc,
                                                       const u16* __restrict__ wq, const u16* __restrict__ wo, u16* __restrict__ out, char* ws) {
  __shared__ __attribute__((aligned(16))) float s_qred[4][16];
  __shared__ __attribute__((aligned(16))) unsigned s_q[256];
  __shared__ __attribute__((aligned(16))) float s_om[4][4][128];
  __shared__ __attribute__((aligned(16))) float s_mm[4][4], s_ll[4][4];
  __shared__ __attribute__((aligned(16))) float s_cred[4][256];
  __shared__ unsigned s_cnt;

  const int b = blockIdx.x, g = b & 7, j = b >> 3;
  const int lane = threadIdx.x & 63, wv = __builtin_amdgcn_readfirstlane(threadIdx.x >> 6);
  const int w = wv & 3, t = threadIdx.x & 255;
  STAMP_DECL
  const __amdgpu_buffer_rsrc_t rs = __builtin_amdgcn_make_buffer_rsrc(ws, 0, WS_END, 0x00020000);

  if (wv < 4) {
    STAMP(0);
    gu64* pE = (gu64*)(ws + WS_E);
    const u64 E = __hip_atomic_load(pE, RLX_AGENT);
    gu64* myO = (gu64*)(ws + WS_H2O) + ((size_t)((4 * g + w) * 32 + j)) * 64 + lane;
    gu64* myML = (gu64*)(ws + WS_H2ML) + (4 * g + w) * 32 + j;
    __hip_atomic_store(myO, 0ull, RLX_AGENT);
    if (lane == 0) __hip_atomic_store(myML, 0ull, RLX_AGENT);
    CBAR();
    const int n0 = g * 512 + j * 16;
    const int half = t & 1, dbase = t >> 1;
    unsigned xv[4];
    u32x4 wqv[4];
#pragma unroll
    for (int i = 0; i < 4; ++i) xv[i] = x[dbase + 128 * i];
#pragma unroll
    for (int i = 0; i < 4; ++i) wqv[i] = ldg16(wq + (size_t)(dbase + 128 * i) * 4096 + n0 + 8 * half);
    if (t == 0) s_cnt = 0u;
    CBAR();
    __syncthreads();
    STAMP(1);
    {
      float acc[8];
#pragma unroll
      for (int k = 0; k < 8; ++k) acc[k] = 0.f;
#pragma unroll
      for (int i = 0; i < 4; ++i) {
        const float xf = bf2f(xv[i]);
#pragma unroll
        for (int k = 0; k < 4; ++k) {
          acc[2 * k] = fmaf(xf, lo16(wqv[i][k]), acc[2 * k]);
          acc[2 * k + 1] = fmaf(xf, hi16(wqv[i][k]), acc[2 * k + 1]);
        }
      }
#pragma unroll
      for (int k = 0; k < 8; ++k) acc[k] = sum_b1to5(acc[k]);
      if (lane < 2) {
        *reinterpret_cast<f32x4*>(&s_qred[w][8 * lane]) = f32x4{acc[0], acc[1], acc[2], acc[3]};
        *reinterpret_cast<f32x4*>(&s_qred[w][8 * lane + 4]) = f32x4{acc[4], acc[5], acc[6], acc[7]};
      }
    }
    const u64 tag48 = (E + 1) & 0xFFFFFFFFFFFFull;
    const u64 tag40 = (E + 1) & 0xFFFFFFFFFFull;
    {
      unsigned old = 0;
      asm volatile("s_waitcnt lgkmcnt(0)" ::: "memory");
      if (lane == 0) old = __hip_atomic_fetch_add(&s_cnt, 1u, __ATOMIC_RELAXED, __HIP_MEMORY_SCOPE_WORKGROUP);
      old = __builtin_amdgcn_readfirstlane(old);
      MEMBAR();
      if (old == 3u && lane < 16) {
        const float qv = (s_qred[0][lane] + s_qred[1][lane]) + (s_qred[2][lane] + s_qred[3][lane]);
        __hip_atomic_store((gu64*)(ws + WS_H1) + n0 + lane, ((u64)f2bf(qv) << 48) | tag48, RLX_AGENT);
      }
    }
    STAMP(2);
    {
      const unsigned off = WS_H1 + (g * 512 + 2 * t) * 8;
      const unsigned tlo = (unsigned)tag48, thi = (unsigned)(tag48 >> 32);
      u32x4 a;
#pragma unroll 1
      for (unsigned sp = 0; sp < SPIN_CAP; ++sp) {
        MEMBAR();
        a = __builtin_amdgcn_raw_buffer_load_b128(rs, off, 0, 16);
        const bool ok = a[0] == tlo && (a[1] & 0xffffu) == thi && a[2] == tlo && (a[3] & 0xffffu) == thi;
        if (__all(ok)) break;
      }
      s_q[t] = (a[1] >> 16) | (a[3] & 0xffff0000u);
    }
    STAMP(3);
    CBAR();
    const int h = 4 * g + (j >> 3), kq = (j >> 1) & 3, ch = j & 1;
    const int r2 = lane >> 5, c32 = lane & 31;
    u32x4 wov[4];
#pragma unroll
    for (int i = 0; i < 4; ++i) wov[i] = ldg16(wo + (size_t)(h * 128 + 32 * kq + 8 * w + 2 * i + r2) * D + ch * 256 + 8 * c32);
    CBAR();
    __syncthreads();
    __syncthreads();
    STAMP(4);
    {
      float mw[4], M = -INFINITY;
#pragma unroll
      for (int i = 0; i < 4; ++i) { mw[i] = s_mm[i][w]; M = fmaxf(M, mw[i]); }
      float L = 0.f, o0 = 0.f, o1 = 0.f;
#pragma unroll
      for (int i = 0; i < 4; ++i) {
        const float e = __builtin_amdgcn_exp2f((mw[i] - M) * LOG2E);
        L = fmaf(e, s_ll[i][w], L);
        const float2 ov = *reinterpret_cast<const float2*>(&s_om[i][w][2 * lane]);
        o0 = fmaf(e, ov.x, o0);
        o1 = fmaf(e, ov.y, o1);
      }
      __hip_atomic_store(myO, (u64)__float_as_uint(o0) | ((u64)(__float_as_uint(o1) | 1u) << 32), RLX_AGENT);
      if (lane == 0) __hip_atomic_store(myML, (u64)__float_as_uint(M) | ((u64)(__float_as_uint(L) | 1u) << 32), RLX_AGENT);
    }
    STAMP(5);
    {
      const int s = lane >> 1, q = lane & 1;
      const unsigned offO = WS_H2O + (unsigned)((h * 32 + s) * 64 + 16 * kq + 4 * w + 2 * q) * 8;
      gu64* srcML = (gu64*)(ws + WS_H2ML) + h * 32 + s;
      u32x4 a; u64 ml;
#pragma unroll 1
      for (unsigned sp = 0; sp < SPIN_CAP; ++sp) {
        MEMBAR();
        a = __builtin_amdgcn_raw_buffer_load_b128(rs, offO, 0, 16);
        ml = __hip_atomic_load(srcML, RLX_AGENT);
        const bool ok = (a[1] & a[3] & (unsigned)(ml >> 32) & 1u) != 0u;
        if (__all(ok)) break;
      }
      STAMP(6);
      const float ms = __uint_as_float((unsigned)ml), ls = __uint_as_float((unsigned)(ml >> 32));
      const float M = max_b1to5(ms);
      const float wsx = __builtin_amdgcn_exp2f((ms - M) * LOG2E);
      const float L = sum_b1to5(wsx * ls);
      const float inv = 1.f / L;
      float own[4], lo[4], hi[4];
#pragma unroll
      for (int k = 0; k < 4; ++k) own[k] = rbf(sum_b1to5(wsx * __uint_as_float(a[k])) * inv);
#pragma unroll
      for (int k = 0; k < 4; ++k) { const float oth = dpp<XOR1>(own[k]); lo[k] = q ? oth : own[k]; hi[k] = q ? own[k] : oth; }
      float acc[8];
#pragma unroll
      for (int k = 0; k < 8; ++k) acc[k] = 0.f;
#pragma unroll
      for (int i = 0; i < 4; ++i) {
        const float of = (i < 2) ? (r2 ? lo[2 * (i & 1) + 1] : lo[2 * (i & 1)]) : (r2 ? hi[2 * (i & 1) + 1] : hi[2 * (i & 1)]);
#pragma unroll
        for (int k = 0; k < 4; ++k) {
          acc[2 * k] = fmaf(of, lo16(wov[i][k]), acc[2 * k]);
          acc[2 * k + 1] = fmaf(of, hi16(wov[i][k]), acc[2 * k + 1]);
        }
      }
      f32x4 r;
#pragma unroll
      for (int k = 0; k < 4; ++k) {
        auto sw = __builtin_amdgcn_permlane32_swap(__float_as_uint(acc[k]), __float_as_uint(acc[k + 4]), false, false);
        r[k] = __uint_as_float(sw[0]) + __uint_as_float(sw[1]);
      }
      *reinterpret_cast<f32x4*>(&s_cred[w][8 * c32 + 4 * r2]) = r;
    }
    __syncthreads();
    {
      const float v = (s_cred[0][t] + s_cred[1][t]) + (s_cred[2][t] + s_cred[3][t]);
      const unsigned fb = (__float_as_uint(v) + 0x80u) >> 8;
      __hip_atomic_store((gu64*)(ws + WS_H3) + (size_t)(h * 4 + kq) * 512 + ch * 256 + t, ((u64)fb << 40) | tag40, RLX_AGENT);
    }
    STAMP(7);
    if (w == 0 && b < 64) {
      const int cp = lane & 3, rr = lane >> 2;
      const unsigned off = WS_H3 + (unsigned)(rr * 512 + 8 * b + 2 * cp) * 8;
      const unsigned tlo = (unsigned)tag40, thi = (unsigned)(tag40 >> 32);
      u32x4 a[8];
#pragma unroll 1
      for (unsigned sp = 0; sp < SPIN_CAP; ++sp) {
        MEMBAR();
        bool ok = true;
#pragma unroll
        for (int i = 0; i < 8; ++i) a[i] = __builtin_amdgcn_raw_buffer_load_b128(rs, off + i * 16 * 512 * 8, 0, 16);
#pragma unroll
        for (int i = 0; i < 8; ++i) ok = ok && a[i][0] == tlo && (a[i][1] & 0xffu) == thi && a[i][2] == tlo && (a[i][3] & 0xffu) == thi;
        if (__all(ok)) break;
      }
      STAMP(8);
      float v0 = 0.f, v1 = 0.f;
#pragma unroll
      for (int i = 0; i < 8; ++i) { v0 += __uint_as_float(a[i][1] & 0xffffff00u); v1 += __uint_as_float(a[i][3] & 0xffffff00u); }
      v0 = sum_b2to5(v0); v1 = sum_b2to5(v1);
      if (lane < 4) {
        *reinterpret_cast<unsigned*>(out + 8 * b + 2 * lane) = f2bf(v0) | (f2bf(v1) << 16);
        if (b == 0 && lane == 0) __hip_atomic_store(pE, E + 1, RLX_AGENT);
      }
      STAMP(9);
    }
    if (w == 0) STAMP_FLUSH(0, 10);
  } else {
    __syncthreads();
    const int kk = lane >> 4, c16 = lane & 15;
    const size_t key0 = (size_t)g * SK + (SK - WIN) + j * 128 + w * 32;
    u32x4 kf[2][4], vr[8];
#pragma unroll
    for (int grp = 0; grp < 2; ++grp)
#pragma unroll
      for (int st = 0; st < 4; ++st) kf[grp][st] = ldg16(kc + (key0 + 16 * grp + c16) * DH + 32 * st + 8 * kk);
#pragma unroll
    for (int i = 0; i < 8; ++i) vr[i] = ldg16(vc + (key0 + 16 * (i >> 2) + 4 * kk + (i & 3)) * DH + 8 * c16);
    CBAR();
    STAMP(10);
    u32x4 bfrag[8];
#pragma unroll
    for (int jj = 0; jj < 8; ++jj)
#pragma unroll
      for (int d2 = 0; d2 < 4; ++d2) {
        const unsigned lo = vr[2 * d2][jj >> 1], hi = vr[2 * d2 + 1][jj >> 1];
        bfrag[jj][d2] = (jj & 1) ? __builtin_amdgcn_perm(hi, lo, 0x07060302u) : __builtin_amdgcn_perm(hi, lo, 0x05040100u);
      }
    CBAR();
    STAMP(11);
    __syncthreads();
    STAMP(12);
    u32x4 qf[4];
#pragma unroll
    for (int st = 0; st < 4; ++st) {
      qf[st] = u32x4{0u, 0u, 0u, 0u};
      if (c16 < 4) qf[st] = *reinterpret_cast<const u32x4*>(&s_q[c16 * 64 + 16 * st + 4 * kk]);
    }
    f32x4 sacc[2];
#pragma unroll
    for (int grp = 0; grp < 2; ++grp) {
      sacc[grp] = f32x4{0.f, 0.f, 0.f, 0.f};
#pragma unroll
      for (int st = 0; st < 4; ++st)
        sacc[grp] = __builtin_amdgcn_mfma_f32_16x16x32_bf16(__builtin_bit_cast(bf16x8, kf[grp][st]), __builtin_bit_cast(bf16x8, qf[st]), sacc[grp], 0, 0, 0);
    }
    float sc[8];
#pragma unroll
    for (int i = 0; i < 8; ++i) sc[i] = rbf(rbf(sacc[i >> 2][i & 3]) * SCALE);
    float m = fmaxf(fmaxf(fmaxf(sc[0], sc[1]), fmaxf(sc[2], sc[3])), fmaxf(fmaxf(sc[4], sc[5]), fmaxf(sc[6], sc[7])));
    m = x32_max(x16_max(m));
    float p[8];
#pragma unroll
    for (int i = 0; i < 8; ++i) p[i] = __builtin_amdgcn_exp2f((sc[i] - m) * LOG2E);
    float l = ((p[0] + p[1]) + (p[2] + p[3])) + ((p[4] + p[5]) + (p[6] + p[7]));
    l = x32_sum(x16_sum(l));
    u32x4 pf;
#pragma unroll
    for (int d2 = 0; d2 < 4; ++d2) pf[d2] = f2bf(p[2 * d2]) | (f2bf(p[2 * d2 + 1]) << 16);
    f32x4 oacc[8];
#pragma unroll
    for (int jj = 0; jj < 8; ++jj)
      oacc[jj] = __builtin_amdgcn_mfma_f32_16x16x32_bf16(__builtin_bit_cast(bf16x8, pf), __builtin_bit_cast(bf16x8, bfrag[jj]), f32x4{0.f, 0.f, 0.f, 0.f}, 0, 0, 0);
    if (lane < 16) {
#pragma unroll
      for (int v = 0; v < 4; ++v) {
        *reinterpret_cast<f32x4*>(&s_om[w][v][8 * lane]) = f32x4{oacc[0][v], oacc[1][v], oacc[2][v], oacc[3][v]};
        *reinterpret_cast<f32x4*>(&s_om[w][v][8 * lane + 4]) = f32x4{oacc[4][v], oacc[5][v], oacc[6][v], oacc[7][v]};
      }
      if (lane < 4) { s_mm[w][lane] = m; s_ll[w][lane] = l; }
    }
    STAMP(13);
    __syncthreads();
    if (w == 0) STAMP_FLUSH(10, 14);
  }
}

extern "C" void kernel_launch(void* const* d_in, const int* in_sizes, int n_in, void* d_out, int out_size, void* d_ws, size_t ws_size, hipStream_t stream) {
  fused_decode<<<256, 512, 0, stream>>>((const u16*)d_in[0], (const u16*)d_in[1], (const u16*)d_in[2], (const u16*)d_in[3], (const u16*)d_in[4],
                                        (u16*)d_out, (char*)d_ws);
}
```

```cpp
#include <hip/hip_runtime.h>
#include <hip/hip_bf16.h>
#include <stdint.h>

typedef unsigned short u16;
typedef unsigned long long u64;
typedef __attribute__((ext_vector_type(8))) __bf16 bf16x8;
typedef __attribute__((ext_vector_type(4))) float f32x4;
typedef __attribute__((ext_vector_type(4))) unsigned u32x4;
typedef __attribute__((ext_vector_type(2))) unsigned u32x2;
typedef __attribute__((ext_vector_type(4))) short s16x4;
#define GAS __attribute__((address_space(1)))
typedef GAS u64 gu64;
#define RLX_AGENT __ATOMIC_RELAXED, __HIP_MEMORY_SCOPE_AGENT

constexpr int D = 512, HQ = 32, HKV = 8, SK = 32768, DH = 128, WIN = 4096;
constexpr unsigned WS_E = 0;
constexpr unsigned WS_H1 = 4096;
constexpr unsigned WS_H2ML = WS_H1 + 32768;
constexpr unsigned WS_H2O = 65536;
constexpr unsigned WS_H3 = WS_H2O + 524288;
constexpr unsigned WS_END = WS_H3 + 524288;
constexpr float LOG2E = 1.4426950408889634f;
constexpr float SCALE = 0.08838834764831845f;
constexpr unsigned SPIN_CAP = 1u << 22;

__device__ __forceinline__ float bf2f(unsigned v) { return __uint_as_float(v << 16); }
__device__ __forceinline__ unsigned f2bf(float f) { __bf16 b = (__bf16)f; return (unsigned)__builtin_bit_cast(unsigned short, b); }
__device__ __forceinline__ float rbf(float f) { return bf2f(f2bf(f)); }
__device__ __forceinline__ u32x4 ldg16(const u16* p) { return __builtin_nontemporal_load(reinterpret_cast<const u32x4*>(p)); }
__device__ __forceinline__ float lo16(unsigned v) { return __uint_as_float(v << 16); }
__device__ __forceinline__ float hi16(unsigned v) { return __uint_as_float(v & 0xffff0000u); }
template <int CTRL> __device__ __forceinline__ float dpp(float x) {
  return __builtin_bit_cast(float, __builtin_amdgcn_mov_dpp(__builtin_bit_cast(int, x), CTRL, 0xf, 0xf, true));
}
constexpr int XOR1 = 0xB1, XOR2 = 0x4E, ROR4 = 0x124, ROR8 = 0x128;
__device__ __forceinline__ float x16_sum(float x) { auto s = __builtin_amdgcn_permlane16_swap(__float_as_uint(x), __float_as_uint(x), false, false); return __uint_as_float(s[0]) + __uint_as_float(s[1]); }
__device__ __forceinline__ float x32_sum(float x) { auto s = __builtin_amdgcn_permlane32_swap(__float_as_uint(x), __float_as_uint(x), false, false); return __uint_as_float(s[0]) + __uint_as_float(s[1]); }
__device__ __forceinline__ float x16_max(float x) { auto s = __builtin_amdgcn_permlane16_swap(__float_as_uint(x), __float_as_uint(x), false, false); return fmaxf(__uint_as_float(s[0]), __uint_as_float(s[1])); }
__device__ __forceinline__ float x32_max(float x) { auto s = __builtin_amdgcn_permlane32_swap(__float_as_uint(x), __float_as_uint(x), false, false); return fmaxf(__uint_as_float(s[0]), __uint_as_float(s[1])); }
__device__ __forceinline__ float sum_b1to5(float x) { x += dpp<XOR2>(x); x += dpp<ROR4>(x); x += dpp<ROR8>(x); x = x16_sum(x); return x32_sum(x); }
__device__ __forceinline__ float max_b1to5(float x) { x = fmaxf(x, dpp<XOR2>(x)); x = fmaxf(x, dpp<ROR4>(x)); x = fmaxf(x, dpp<ROR8>(x)); x = x16_max(x); return x32_max(x); }
__device__ __forceinline__ float sum_b2to5(float x) { x += dpp<ROR4>(x); x += dpp<ROR8>(x); x = x16_sum(x); return x32_sum(x); }

#define STAMP_DECL
#define STAMP(i) do {} while (0)
#define STAMP_FLUSH(lo, hi) do {} while (0)
#define CBAR() do { asm volatile("" ::: "memory"); __builtin_amdgcn_sched_barrier(0); } while (0)
#define MEMBAR() asm volatile("" ::: "memory")

__global__ __launch_bounds__(768, 1) void fused_decode(const u16* __restrict__ x, const u16* __restrict__ kc, const u16* __restrict__ vc,
                                                       const u16* __restrict__ wq, const u16* __restrict__ wo, u16* __restrict__ out, char* ws) {
  __shared__ __attribute__((aligned(16))) float s_qred[4][16];
  __shared__ __attribute__((aligned(16))) unsigned s_q[256];
  __shared__ __attribute__((aligned(16))) float s_om[8][4][128];
  __shared__ __attribute__((aligned(16))) float s_mm[4][8], s_ll[4][8];
  __shared__ __attribute__((aligned(16))) float s_cred[4][256];
  __shared__ unsigned s_cnt;

  const int b = blockIdx.x, g = b & 7, j = b >> 3;
  const int lane = threadIdx.x & 63, wv = __builtin_amdgcn_readfirstlane(threadIdx.x >> 6);
  const int w = wv < 4 ? wv : wv - 4, t = threadIdx.x & 255;
  STAMP_DECL
  const __amdgpu_buffer_rsrc_t rs = __builtin_amdgcn_make_buffer_rsrc(ws, 0, WS_END, 0x00020000);

  if (wv < 4) {
    STAMP(0);
    gu64* pE = (gu64*)(ws + WS_E);
    const u64 E = __hip_atomic_load(pE, RLX_AGENT);
    gu64* myO = (gu64*)(ws + WS_H2O) + ((size_t)((4 * g + w) * 32 + j)) * 64 + lane;
    gu64* myML = (gu64*)(ws + WS_H2ML) + (4 * g + w) * 32 + j;
    __hip_atomic_store(myO, 0ull, RLX_AGENT);
    if (lane == 0) __hip_atomic_store(myML, 0ull, RLX_AGENT);
    CBAR();
    const int n0 = g * 512 + j * 16;
    const int half = t & 1, dbase = t >> 1;
    unsigned xv[4];
    u32x4 wqv[4];
#pragma unroll
    for (int i = 0; i < 4; ++i) xv[i] = x[dbase + 128 * i];
#pragma unroll
    for (int i = 0; i < 4; ++i) wqv[i] = ldg16(wq + (size_t)(dbase + 128 * i) * 4096 + n0 + 8 * half);
    if (t == 0) s_cnt = 0u;
    CBAR();
    __syncthreads();
    STAMP(1);
    {
      float acc[8];
#pragma unroll
      for (int k = 0; k < 8; ++k) acc[k] = 0.f;
#pragma unroll
      for (int i = 0; i < 4; ++i) {
        const float xf = bf2f(xv[i]);
#pragma unroll
        for (int k = 0; k < 4; ++k) {
          acc[2 * k] = fmaf(xf, lo16(wqv[i][k]), acc[2 * k]);
          acc[2 * k + 1] = fmaf(xf, hi16(wqv[i][k]), acc[2 * k + 1]);
        }
      }
#pragma unroll
      for (int k = 0; k < 8; ++k) acc[k] = sum_b1to5(acc[k]);
      if (lane < 2) {
        *reinterpret_cast<f32x4*>(&s_qred[w][8 * lane]) = f32x4{acc[0], acc[1], acc[2], acc[3]};
        *reinterpret_cast<f32x4*>(&s_qred[w][8 * lane + 4]) = f32x4{acc[4], acc[5], acc[6], acc[7]};
      }
    }
    const u64 tag48 = (E + 1) & 0xFFFFFFFFFFFFull;
    const u64 tag40 = (E + 1) & 0xFFFFFFFFFFull;
    {
      unsigned old = 0;
      asm volatile("s_waitcnt lgkmcnt(0)" ::: "memory");
      if (lane == 0) old = __hip_atomic_fetch_add(&s_cnt, 1u, __ATOMIC_RELAXED, __HIP_MEMORY_SCOPE_WORKGROUP);
      old = __builtin_amdgcn_readfirstlane(old);
      MEMBAR();
      if (old == 3u && lane < 16) {
        const float qv = (s_qred[0][lane] + s_qred[1][lane]) + (s_qred[2][lane] + s_qred[3][lane]);
        __hip_atomic_store((gu64*)(ws + WS_H1) + n0 + lane, ((u64)f2bf(qv) << 48) | tag48, RLX_AGENT);
      }
    }
    STAMP(2);
    __syncthreads();
    {
      const unsigned off = WS_H1 + (g * 512 + 2 * t) * 8;
      const unsigned tlo = (unsigned)tag48, thi = (unsigned)(tag48 >> 32);
      u32x4 a;
#pragma unroll 1
      for (unsigned sp = 0; sp < SPIN_CAP; ++sp) {
        MEMBAR();
        a = __builtin_amdgcn_raw_buffer_load_b128(rs, off, 0, 16);
        const bool ok = a[0] == tlo && (a[1] & 0xffffu) == thi && a[2] == tlo && (a[3] & 0xffffu) == thi;
        if (__all(ok)) break;
      }
      s_q[t] = (a[1] >> 16) | (a[3] & 0xffff0000u);
    }
    STAMP(3);
    CBAR();
    const int h = 4 * g + (j >> 3), kq = (j >> 1) & 3, ch = j & 1;
    const int r2 = lane >> 5, c32 = lane & 31;
    u32x4 wov[4];
#pragma unroll
    for (int i = 0; i < 4; ++i) wov[i] = ldg16(wo + (size_t)(h * 128 + 32 * kq + 8 * w + 2 * i + r2) * D + ch * 256 + 8 * c32);
    CBAR();
    __syncthreads();
    __syncthreads();
    STAMP(4);
    {
      float mw[8], lw[8];
      *reinterpret_cast<f32x4*>(&mw[0]) = *reinterpret_cast<const f32x4*>(&s_mm[w][0]);
      *reinterpret_cast<f32x4*>(&mw[4]) = *reinterpret_cast<const f32x4*>(&s_mm[w][4]);
      *reinterpret_cast<f32x4*>(&lw[0]) = *reinterpret_cast<const f32x4*>(&s_ll[w][0]);
      *reinterpret_cast<f32x4*>(&lw[4]) = *reinterpret_cast<const f32x4*>(&s_ll[w][4]);
      float2 ov[8];
#pragma unroll
      for (int i = 0; i < 8; ++i) ov[i] = *reinterpret_cast<const float2*>(&s_om[i][w][2 * lane]);
      const float M = fmaxf(fmaxf(fmaxf(mw[0], mw[1]), fmaxf(mw[2], mw[3])), fmaxf(fmaxf(mw[4], mw[5]), fmaxf(mw[6], mw[7])));
      float L = 0.f, o0 = 0.f, o1 = 0.f;
#pragma unroll
      for (int i = 0; i < 8; ++i) {
        const float e = __builtin_amdgcn_exp2f((mw[i] - M) * LOG2E);
        L = fmaf(e, lw[i], L);
        o0 = fmaf(e, ov[i].x, o0);
        o1 = fmaf(e, ov[i].y, o1);
      }
      __hip_atomic_store(myO, (u64)__float_as_uint(o0) | ((u64)(__float_as_uint(o1) | 1u) << 32), RLX_AGENT);
      if (lane == 0) __hip_atomic_store(myML, (u64)__float_as_uint(M) | ((u64)(__float_as_uint(L) | 1u) << 32), RLX_AGENT);
    }
    STAMP(5);
    {
      const int s = lane >> 1, q = lane & 1;
      const unsigned offO = WS_H2O + (unsigned)((h * 32 + s) * 64 + 16 * kq + 4 * w + 2 * q) * 8;
      gu64* srcML = (gu64*)(ws + WS_H2ML) + h * 32 + s;
      u32x4 a; u64 ml;
#pragma unroll 1
      for (unsigned sp = 0; sp < SPIN_CAP; ++sp) {
        MEMBAR();
        a = __builtin_amdgcn_raw_buffer_load_b128(rs, offO, 0, 16);
        ml = __hip_atomic_load(srcML, RLX_AGENT);
        const bool ok = (a[1] & a[3] & (unsigned)(ml >> 32) & 1u) != 0u;
        if (__all(ok)) break;
      }
      STAMP(6);
      const float ms = __uint_as_float((unsigned)ml), ls = __uint_as_float((unsigned)(ml >> 32));
      const float M = max_b1to5(ms);
      const float wsx = __builtin_amdgcn_exp2f((ms - M) * LOG2E);
      const float L = sum_b1to5(wsx * ls);
      const float inv = 1.f / L;
      float own[4], lo[4], hi[4];
#pragma unroll
      for (int k = 0; k < 4; ++k) own[k] = rbf(sum_b1to5(wsx * __uint_as_float(a[k])) * inv);
#pragma unroll
      for (int k = 0; k < 4; ++k) { const float oth = dpp<XOR1>(own[k]); lo[k] = q ? oth : own[k]; hi[k] = q ? own[k] : oth; }
      float acc[8];
#pragma unroll
      for (int k = 0; k < 8; ++k) acc[k] = 0.f;
#pragma unroll
      for (int i = 0; i < 4; ++i) {
        const float of = (i < 2) ? (r2 ? lo[2 * (i & 1) + 1] : lo[2 * (i & 1)]) : (r2 ? hi[2 * (i & 1) + 1] : hi[2 * (i & 1)]);
#pragma unroll
        for (int k = 0; k < 4; ++k) {
          acc[2 * k] = fmaf(of, lo16(wov[i][k]), acc[2 * k]);
          acc[2 * k + 1] = fmaf(of, hi16(wov[i][k]), acc[2 * k + 1]);
        }
      }
      f32x4 r;
#pragma unroll
      for (int k = 0; k < 4; ++k) {
        auto sw = __builtin_amdgcn_permlane32_swap(__float_as_uint(acc[k]), __float_as_uint(acc[k + 4]), false, false);
        r[k] = __uint_as_float(sw[0]) + __uint_as_float(sw[1]);
      }
      *reinterpret_cast<f32x4*>(&s_cred[w][8 * c32 + 4 * r2]) = r;
    }
    __syncthreads();
    {
      const float v = (s_cred[0][t] + s_cred[1][t]) + (s_cred[2][t] + s_cred[3][t]);
      const unsigned fb = (__float_as_uint(v) + 0x80u) >> 8;
      __hip_atomic_store((gu64*)(ws + WS_H3) + (size_t)(h * 4 + kq) * 512 + ch * 256 + t, ((u64)fb << 40) | tag40, RLX_AGENT);
    }
    STAMP(7);
    if (w == 0 && b < 64) {
      const int cp = lane & 3, rr = lane >> 2;
      const unsigned off = WS_H3 + (unsigned)(rr * 512 + 8 * b + 2 * cp) * 8;
      const unsigned tlo = (unsigned)tag40, thi = (unsigned)(tag40 >> 32);
      u32x4 a[8];
#pragma unroll 1
      for (unsigned sp = 0; sp < SPIN_CAP; ++sp) {
        MEMBAR();
        bool ok = true;
#pragma unroll
        for (int i = 0; i < 8; ++i) a[i] = __builtin_amdgcn_raw_buffer_load_b128(rs, off + i * 16 * 512 * 8, 0, 16);
#pragma unroll
        for (int i = 0; i < 8; ++i) ok = ok && a[i][0] == tlo && (a[i][1] & 0xffu) == thi && a[i][2] == tlo && (a[i][3] & 0xffu) == thi;
        if (__all(ok)) break;
      }
      STAMP(8);
      float v0 = 0.f, v1 = 0.f;
#pragma unroll
      for (int i = 0; i < 8; ++i) { v0 += __uint_as_float(a[i][1] & 0xffffff00u); v1 += __uint_as_float(a[i][3] & 0xffffff00u); }
      v0 = sum_b2to5(v0); v1 = sum_b2to5(v1);
      if (lane < 4) {
        *reinterpret_cast<unsigned*>(out + 8 * b + 2 * lane) = f2bf(v0) | (f2bf(v1) << 16);
        if (b == 0 && lane == 0) __hip_atomic_store(pE, E + 1, RLX_AGENT);
      }
      STAMP(9);
    }
    if (w == 0) STAMP_FLUSH(0, 10);
  } else {
    __syncthreads();
    const int kk = lane >> 4, c16 = lane & 15;
    const size_t key0 = (size_t)g * SK + (SK - WIN) + j * 128 + w * 16;
    u32x4 kf[4], vr[4];
#pragma unroll
    for (int st = 0; st < 4; ++st) kf[st] = ldg16(kc + (key0 + c16) * DH + 32 * st + 8 * kk);
#pragma unroll
    for (int i = 0; i < 4; ++i) vr[i] = ldg16(vc + (key0 + 4 * kk + i) * DH + 8 * c16);
    CBAR();
    STAMP(10);
    u32x2 bfrag[8];
#pragma unroll
    for (int jj = 0; jj < 8; ++jj) {
#pragma unroll
      for (int d2 = 0; d2 < 2; ++d2) {
        const unsigned lo = vr[2 * d2][jj >> 1], hi = vr[2 * d2 + 1][jj >> 1];
        bfrag[jj][d2] = (jj & 1) ? __builtin_amdgcn_perm(hi, lo, 0x07060302u) : __builtin_amdgcn_perm(hi, lo, 0x05040100u);
      }
      asm volatile("" : "+v"(bfrag[jj]));
    }
#pragma unroll
    for (int st = 0; st < 4; ++st) asm volatile("" : "+v"(kf[st]));
    CBAR();
    STAMP(11);
    __syncthreads();
    __syncthreads();
    STAMP(12);
    u32x4 qf[4];
#pragma unroll
    for (int st = 0; st < 4; ++st) {
      qf[st] = u32x4{0u, 0u, 0u, 0u};
      if (c16 < 4) qf[st] = *reinterpret_cast<const u32x4*>(&s_q[c16 * 64 + 16 * st + 4 * kk]);
    }
    f32x4 sacc = f32x4{0.f, 0.f, 0.f, 0.f};
#pragma unroll
    for (int st = 0; st < 4; ++st)
      sacc = __builtin_amdgcn_mfma_f32_16x16x32_bf16(__builtin_bit_cast(bf16x8, kf[st]), __builtin_bit_cast(bf16x8, qf[st]), sacc, 0, 0, 0);
    float sc[4];
#pragma unroll
    for (int i = 0; i < 4; ++i) sc[i] = rbf(rbf(sacc[i]) * SCALE);
    float m = fmaxf(fmaxf(sc[0], sc[1]), fmaxf(sc[2], sc[3]));
    m = x32_max(x16_max(m));
    float p[4];
#pragma unroll
    for (int i = 0; i < 4; ++i) p[i] = __builtin_amdgcn_exp2f((sc[i] - m) * LOG2E);
    float l = (p[0] + p[1]) + (p[2] + p[3]);
    l = x32_sum(x16_sum(l));
    u32x2 pf;
#pragma unroll
    for (int d2 = 0; d2 < 2; ++d2) pf[d2] = f2bf(p[2 * d2]) | (f2bf(p[2 * d2 + 1]) << 16);
    f32x4 oacc[8];
#pragma unroll
    for (int jj = 0; jj < 8; ++jj)
      oacc[jj] = __builtin_amdgcn_mfma_f32_16x16x16bf16_1k(__builtin_bit_cast(s16x4, pf), __builtin_bit_cast(s16x4, bfrag[jj]), f32x4{0.f, 0.f, 0.f, 0.f}, 0, 0, 0);
    if (lane < 16) {
#pragma unroll
      for (int v = 0; v < 4; ++v) {
        *reinterpret_cast<f32x4*>(&s_om[w][v][8 * lane]) = f32x4{oacc[0][v], oacc[1][v], oacc[2][v], oacc[3][v]};
        *reinterpret_cast<f32x4*>(&s_om[w][v][8 * lane + 4]) = f32x4{oacc[4][v], oacc[5][v], oacc[6][v], oacc[7][v]};
      }
      if (lane < 4) { s_mm[lane][w] = m; s_ll[lane][w] = l; }
    }
    STAMP(13);
    __syncthreads();
    if (w == 0) STAMP_FLUSH(10, 14);
  }
}

extern "C" void kernel_launch(void* const* d_in, const int* in_sizes, int n_in, void* d_out, int out_size, void* d_ws, size_t ws_size, hipStream_t stream) {
  fused_decode<<<256, 768, 0, stream>>>((const u16*)d_in[0], (const u16*)d_in[1], (const u16*)d_in[2], (const u16*)d_in[3], (const u16*)d_in[4],
                                        (u16*)d_out, (char*)d_ws);
}
```

```cpp
#include <hip/hip_runtime.h>
#include <hip/hip_bf16.h>
#include <stdint.h>

typedef unsigned short u16;
typedef unsigned long long u64;
typedef __attribute__((ext_vector_type(8))) __bf16 bf16x8;
typedef __attribute__((ext_vector_type(4))) float f32x4;
typedef __attribute__((ext_vector_type(4))) unsigned u32x4;
typedef __attribute__((ext_vector_type(2))) unsigned u32x2;
typedef __attribute__((ext_vector_type(4))) short s16x4;
#define GAS __attribute__((address_space(1)))
#define LAS __attribute__((address_space(3)))
typedef GAS u64 gu64;
typedef GAS unsigned gu32;
#define RLX_AGENT __ATOMIC_RELAXED, __HIP_MEMORY_SCOPE_AGENT

constexpr int D = 512, HQ = 32, HKV = 8, SK = 32768, DH = 128, WIN = 4096;
constexpr unsigned WS_E = 0;
constexpr unsigned WS_H2ML = 4096;
constexpr unsigned WS_H2O = 65536;
constexpr unsigned WS_H3 = WS_H2O + 524288;
constexpr unsigned WS_CNT = WS_H3 + 524288;
constexpr unsigned WS_H1 = WS_CNT + 3 * 256 * 256;
constexpr unsigned WS_CNT3 = WS_H1 + 131072;
constexpr unsigned WS_END = WS_CNT3 + 64 * 8 * 128;
constexpr float LOG2E = 1.4426950408889634f;
constexpr float SCALE = 0.08838834764831845f;
constexpr unsigned SPIN_CAP = 1u << 22;
constexpr unsigned HINT_CAP = 24;

__device__ __forceinline__ float bf2f(unsigned v) { return __uint_as_float(v << 16); }
__device__ __forceinline__ unsigned f2bf(float f) { __bf16 b = (__bf16)f; return (unsigned)__builtin_bit_cast(unsigned short, b); }
__device__ __forceinline__ float rbf(float f) { return bf2f(f2bf(f)); }
__device__ __forceinline__ u32x4 ldg16(const u16* p) { return __builtin_nontemporal_load(reinterpret_cast<const u32x4*>(p)); }
__device__ __forceinline__ float lo16(unsigned v) { return __uint_as_float(v << 16); }
__device__ __forceinline__ float hi16(unsigned v) { return __uint_as_float(v & 0xffff0000u); }
template <int CTRL> __device__ __forceinline__ float dpp(float x) {
  return __builtin_bit_cast(float, __builtin_amdgcn_mov_dpp(__builtin_bit_cast(int, x), CTRL, 0xf, 0xf, true));
}
constexpr int XOR1 = 0xB1, XOR2 = 0x4E, ROR4 = 0x124, ROR8 = 0x128;
__device__ __forceinline__ float x16_sum(float x) { auto s = __builtin_amdgcn_permlane16_swap(__float_as_uint(x), __float_as_uint(x), false, false); return __uint_as_float(s[0]) + __uint_as_float(s[1]); }
__device__ __forceinline__ float x32_sum(float x) { auto s = __builtin_amdgcn_permlane32_swap(__float_as_uint(x), __float_as_uint(x), false, false); return __uint_as_float(s[0]) + __uint_as_float(s[1]); }
__device__ __forceinline__ float x16_max(float x) { auto s = __builtin_amdgcn_permlane16_swap(__float_as_uint(x), __float_as_uint(x), false, false); return fmaxf(__uint_as_float(s[0]), __uint_as_float(s[1])); }
__device__ __forceinline__ float x32_max(float x) { auto s = __builtin_amdgcn_permlane32_swap(__float_as_uint(x), __float_as_uint(x), false, false); return fmaxf(__uint_as_float(s[0]), __uint_as_float(s[1])); }
__device__ __forceinline__ float sum_b1to5(float x) { x += dpp<XOR2>(x); x += dpp<ROR4>(x); x += dpp<ROR8>(x); x = x16_sum(x); return x32_sum(x); }
__device__ __forceinline__ float max_b1to5(float x) { x = fmaxf(x, dpp<XOR2>(x)); x = fmaxf(x, dpp<ROR4>(x)); x = fmaxf(x, dpp<ROR8>(x)); x = x16_max(x); return x32_max(x); }
__device__ __forceinline__ float sum_b2to5(float x) { x += dpp<ROR4>(x); x += dpp<ROR8>(x); x = x16_sum(x); return x32_sum(x); }

#define STAMP_DECL
#define STAMP(i) do {} while (0)
#define STAMP_FLUSH(lo, hi) do {} while (0)
#define CBAR() do { asm volatile("" ::: "memory"); __builtin_amdgcn_sched_barrier(0); } while (0)
#define MEMBAR() asm volatile("" ::: "memory")
#define SYNC() do { __builtin_amdgcn_sched_barrier(0); __syncthreads(); __builtin_amdgcn_sched_barrier(0); } while (0)

__device__ __forceinline__ void hint_wait(gu32* c, unsigned n, int slp) {
#pragma unroll 1
  for (unsigned sp = 0; sp < HINT_CAP; ++sp) {
    MEMBAR();
    const unsigned v = __hip_atomic_load(c, RLX_AGENT);
    if (__builtin_amdgcn_readfirstlane(v) == n) break;
    if (slp) __builtin_amdgcn_s_sleep(2);
  }
}

__device__ __forceinline__ void hint_wait8(gu32* c, unsigned n, int lane) {
#pragma unroll 1
  for (unsigned sp = 0; sp < HINT_CAP; ++sp) {
    MEMBAR();
    const unsigned v = __hip_atomic_load(c + (lane & 7) * 32, RLX_AGENT);
    if (__all(v == n)) break;
  }
}

__global__ __launch_bounds__(768, 1) void fused_decode(const u16* __restrict__ x, const u16* __restrict__ kc, const u16* __restrict__ vc,
                                                       const u16* __restrict__ wq, const u16* __restrict__ wo, u16* __restrict__ out, char* ws) {
  __shared__ __attribute__((aligned(16))) float s_qred[4][64];
  __shared__ __attribute__((aligned(16))) unsigned s_q[4 * 80];
  __shared__ __attribute__((aligned(16))) float s_om[8][4][128];
  __shared__ __attribute__((aligned(16))) float s_mm[4][8], s_ll[4][8];
  __shared__ __attribute__((aligned(16))) float s_cred[4][256];
  __shared__ __attribute__((aligned(16))) unsigned s_flag[4];

  const int b = blockIdx.x, g = b & 7, j = b >> 3;
  const int lane = threadIdx.x & 63, wv = __builtin_amdgcn_readfirstlane(threadIdx.x >> 6);
  const int w = wv < 4 ? wv : wv - 4, t = threadIdx.x & 255;
  STAMP_DECL
  const __amdgpu_buffer_rsrc_t rs = __builtin_amdgcn_make_buffer_rsrc(ws, 0, WS_END, 0x00020000);
  gu32* CNT = (gu32*)(ws + WS_CNT);

  if (wv < 4) {
    __builtin_amdgcn_s_setprio(3);
    asm volatile("" :: "s"(wo), "s"(out));
    STAMP(0);
    gu64* pE = (gu64*)(ws + WS_E);
    const u64 E = *reinterpret_cast<const u64* __restrict__>(ws + WS_E);
    const int dq = j >> 3, cb = j & 7;
    const int c8 = t & 7, rbase = 128 * dq + (t >> 3);
    unsigned xv[4];
    u32x4 wqv[4];
#pragma unroll
    for (int i = 0; i < 4; ++i) xv[i] = x[rbase + 32 * i];
#pragma unroll
    for (int i = 0; i < 4; ++i) wqv[i] = ldg16(wq + (size_t)(rbase + 32 * i) * 4096 + g * 512 + cb * 64 + 8 * c8);
    CBAR();
    gu64* myO = (gu64*)(ws + WS_H2O) + ((size_t)((4 * g + w) * 32 + j)) * 64 + lane;
    gu64* myML = (gu64*)(ws + WS_H2ML) + (4 * g + w) * 32 + j;
    __hip_atomic_store(myO, 0ull, RLX_AGENT);
    if (lane == 0) __hip_atomic_store(myML, 0ull, RLX_AGENT);
    gu32* CNT3 = (gu32*)(ws + WS_CNT3);
    if (t == 0) __hip_atomic_store(CNT + (256 + b) * 64, 0u, RLX_AGENT);
    if (b < 64 && t >= 64 && t < 72) __hip_atomic_store(CNT3 + (b * 8 + (t & 7)) * 32, 0u, RLX_AGENT);
    CBAR();
    SYNC();
    STAMP(1);
    {
      float acc[8];
#pragma unroll
      for (int k = 0; k < 8; ++k) acc[k] = 0.f;
#pragma unroll
      for (int i = 0; i < 4; ++i) {
        const float xf = bf2f(xv[i]);
#pragma unroll
        for (int k = 0; k < 4; ++k) {
          acc[2 * k] = fmaf(xf, lo16(wqv[i][k]), acc[2 * k]);
          acc[2 * k + 1] = fmaf(xf, hi16(wqv[i][k]), acc[2 * k + 1]);
        }
      }
#pragma unroll
      for (int k = 0; k < 8; ++k) { float v = acc[k]; v += dpp<ROR8>(v); v = x16_sum(v); acc[k] = x32_sum(v); }
      if (lane < 8) {
        *reinterpret_cast<f32x4*>(&s_qred[w][8 * lane]) = f32x4{acc[0], acc[1], acc[2], acc[3]};
        *reinterpret_cast<f32x4*>(&s_qred[w][8 * lane + 4]) = f32x4{acc[4], acc[5], acc[6], acc[7]};
      }
    }
    const u64 tag40 = (E + 1) & 0xFFFFFFFFFFull;
    {
      const unsigned ftag = (unsigned)(E + 1) ^ 0x9e3779b9u;
      STAMP(14);
      asm volatile("s_waitcnt vmcnt(0)" ::: "memory");
      STAMP(15);
      if (lane == 0) *(volatile LAS unsigned*)(&s_flag[w]) = ftag;
      asm volatile("s_waitcnt lgkmcnt(0)" ::: "memory");
      const u32x4 fl = *(volatile LAS u32x4*)(&s_flag[0]);
      MEMBAR();
      if (fl[0] == ftag && fl[1] == ftag && fl[2] == ftag && fl[3] == ftag) {
        const float qv = (s_qred[0][lane] + s_qred[1][lane]) + (s_qred[2][lane] + s_qred[3][lane]);
        const unsigned fb = (__float_as_uint(qv) + 0x80u) >> 8;
        __hip_atomic_store((gu64*)(ws + WS_H1) + (g * 4 + dq) * 512 + cb * 64 + lane, ((u64)fb << 40) | tag40, RLX_AGENT);
      }
    }
    STAMP(2);
    SYNC();
    {
      const unsigned off = WS_H1 + (g * 2048 + 2 * t) * 8;
      const unsigned tlo = (unsigned)tag40, thi = (unsigned)(tag40 >> 32);
      u32x4 a[4];
#pragma unroll 1
      for (unsigned sp = 0; sp < SPIN_CAP; ++sp) {
        MEMBAR();
        bool ok = true;
#pragma unroll
        for (int i = 0; i < 4; ++i) a[i] = __builtin_amdgcn_raw_buffer_load_b128(rs, off + i * 4096, 0, 16);
#pragma unroll
        for (int i = 0; i < 4; ++i) ok = ok & (a[i][0] == tlo) & ((a[i][1] & 0xffu) == thi) & (a[i][2] == tlo) & ((a[i][3] & 0xffu) == thi);
        if (__all(ok)) break;
      }
      const float q0 = (__uint_as_float(a[0][1] & 0xffffff00u) + __uint_as_float(a[1][1] & 0xffffff00u)) + (__uint_as_float(a[2][1] & 0xffffff00u) + __uint_as_float(a[3][1] & 0xffffff00u));
      const float q1 = (__uint_as_float(a[0][3] & 0xffffff00u) + __uint_as_float(a[1][3] & 0xffffff00u)) + (__uint_as_float(a[2][3] & 0xffffff00u) + __uint_as_float(a[3][3] & 0xffffff00u));
      s_q[w * 80 + lane] = f2bf(q0) | (f2bf(q1) << 16);
    }
    STAMP(3);
    SYNC();
    CBAR();
    const int hh = j >> 3, h = 4 * g + hh, kq = (j >> 1) & 3, ch = j & 1;
    const int r2 = lane >> 5, c32 = lane & 31;
    u32x4 wov[4];
#pragma unroll
    for (int i = 0; i < 4; ++i) wov[i] = ldg16(wo + (size_t)(h * 128 + 32 * kq + 8 * w + 2 * i + r2) * D + ch * 256 + 8 * c32);
    CBAR();
    SYNC();
    STAMP(4);
    {
      float mw[8], lw[8];
      *reinterpret_cast<f32x4*>(&mw[0]) = *reinterpret_cast<const f32x4*>(&s_mm[w][0]);
      *reinterpret_cast<f32x4*>(&mw[4]) = *reinterpret_cast<const f32x4*>(&s_mm[w][4]);
      *reinterpret_cast<f32x4*>(&lw[0]) = *reinterpret_cast<const f32x4*>(&s_ll[w][0]);
      *reinterpret_cast<f32x4*>(&lw[4]) = *reinterpret_cast<const f32x4*>(&s_ll[w][4]);
      float ov0[8], ov1[8];
#pragma unroll
      for (int i = 0; i < 8; ++i) { const float2 o2 = *reinterpret_cast<const float2*>(&s_om[i][w][2 * lane]); ov0[i] = o2.x; ov1[i] = o2.y; }
      const float M = fmaxf(fmaxf(fmaxf(mw[0], mw[1]), fmaxf(mw[2], mw[3])), fmaxf(fmaxf(mw[4], mw[5]), fmaxf(mw[6], mw[7])));
      float L = 0.f, o0 = 0.f, o1 = 0.f;
#pragma unroll
      for (int i = 0; i < 8; ++i) {
        const float e = __builtin_amdgcn_exp2f((mw[i] - M) * LOG2E);
        L = fmaf(e, lw[i], L);
        o0 = fmaf(e, ov0[i], o0);
        o1 = fmaf(e, ov1[i], o1);
      }
      __hip_atomic_store(myO, (u64)__float_as_uint(o0) | ((u64)(__float_as_uint(o1) | 1u) << 32), RLX_AGENT);
      if (lane == 0) __hip_atomic_store(myML, (u64)__float_as_uint(M) | ((u64)(__float_as_uint(L) | 1u) << 32), RLX_AGENT);
      MEMBAR();
      if (lane < 8) __hip_atomic_fetch_add(CNT + (256 + (8 * w + lane) * 8 + g) * 64, 1u, RLX_AGENT);
    }
    STAMP(5);
    float wf[4][8];
#pragma unroll
    for (int i = 0; i < 4; ++i)
#pragma unroll
      for (int k = 0; k < 4; ++k) { wf[i][2 * k] = lo16(wov[i][k]); wf[i][2 * k + 1] = hi16(wov[i][k]); }
#pragma unroll
    for (int i = 0; i < 4; ++i)
#pragma unroll
      for (int k = 0; k < 8; ++k) asm volatile("" : "+v"(wf[i][k]));
    CBAR();
    if (w == 0) hint_wait(CNT + (256 + b) * 64, 32u, 0);
    SYNC();
    {
      const int s = lane >> 1, q = lane & 1;
      const unsigned offO = WS_H2O + (unsigned)((h * 32 + s) * 64 + 16 * kq + 4 * w + 2 * q) * 8;
      gu64* srcML = (gu64*)(ws + WS_H2ML) + h * 32 + s;
      u32x4 a; u64 ml;
#pragma unroll 1
      for (unsigned sp = 0; sp < SPIN_CAP; ++sp) {
        MEMBAR();
        a = __builtin_amdgcn_raw_buffer_load_b128(rs, offO, 0, 16);
        ml = __hip_atomic_load(srcML, RLX_AGENT);
        const bool ok = (a[1] & a[3] & (unsigned)(ml >> 32) & 1u) != 0u;
        if (__all(ok)) break;
      }
      STAMP(6);
      const float ms = __uint_as_float((unsigned)ml), ls = __uint_as_float((unsigned)(ml >> 32));
      const float M = max_b1to5(ms);
      const float wsx = __builtin_amdgcn_exp2f((ms - M) * LOG2E);
      float v5[5];
#pragma unroll
      for (int k = 0; k < 4; ++k) v5[k] = wsx * __uint_as_float(a[k]);
      v5[4] = wsx * ls;
#pragma unroll
      for (int k = 0; k < 5; ++k) v5[k] += dpp<XOR2>(v5[k]);
#pragma unroll
      for (int k = 0; k < 5; ++k) v5[k] += dpp<ROR4>(v5[k]);
#pragma unroll
      for (int k = 0; k < 5; ++k) v5[k] += dpp<ROR8>(v5[k]);
#pragma unroll
      for (int k = 0; k < 5; ++k) v5[k] = x16_sum(v5[k]);
#pragma unroll
      for (int k = 0; k < 5; ++k) v5[k] = x32_sum(v5[k]);
      const float inv = __builtin_amdgcn_rcpf(v5[4]);
      float own[4], lo[4], hi[4];
#pragma unroll
      for (int k = 0; k < 4; ++k) own[k] = rbf(v5[k] * inv);
#pragma unroll
      for (int k = 0; k < 4; ++k) { const float oth = dpp<XOR1>(own[k]); lo[k] = q ? oth : own[k]; hi[k] = q ? own[k] : oth; }
      float acc[8];
#pragma unroll
      for (int k = 0; k < 8; ++k) acc[k] = 0.f;
#pragma unroll
      for (int i = 0; i < 4; ++i) {
        const float of = (i < 2) ? (r2 ? lo[2 * (i & 1) + 1] : lo[2 * (i & 1)]) : (r2 ? hi[2 * (i & 1) + 1] : hi[2 * (i & 1)]);
#pragma unroll
        for (int k = 0; k < 8; ++k) acc[k] = fmaf(of, wf[i][k], acc[k]);
      }
      f32x4 r;
#pragma unroll
      for (int k = 0; k < 4; ++k) {
        auto sw = __builtin_amdgcn_permlane32_swap(__float_as_uint(acc[k]), __float_as_uint(acc[k + 4]), false, false);
        r[k] = __uint_as_float(sw[0]) + __uint_as_float(sw[1]);
      }
      *reinterpret_cast<f32x4*>(&s_cred[w][8 * c32 + 4 * r2]) = r;
    }
    SYNC();
    {
      const float v = (s_cred[0][t] + s_cred[1][t]) + (s_cred[2][t] + s_cred[3][t]);
      const unsigned fb = (__float_as_uint(v) + 0x80u) >> 8;
      __hip_atomic_store((gu64*)(ws + WS_H3) + (size_t)(h * 4 + kq) * 512 + ch * 256 + t, ((u64)fb << 40) | tag40, RLX_AGENT);
      MEMBAR();

    }
    STAMP(7);
    if (w == 0 && b < 64) {

      const int cp = lane & 3, rr = lane >> 2;
      const unsigned off = WS_H3 + (unsigned)(rr * 512 + 8 * b + 2 * cp) * 8;
      const unsigned tlo = (unsigned)tag40, thi = (unsigned)(tag40 >> 32);
      u32x4 a[8];
#pragma unroll 1
      for (unsigned sp = 0; sp < SPIN_CAP; ++sp) {
        MEMBAR();
        bool ok = true;
#pragma unroll
        for (int i = 0; i < 8; ++i) a[i] = __builtin_amdgcn_raw_buffer_load_b128(rs, off + i * 16 * 512 * 8, 0, 16);
#pragma unroll
        for (int i = 0; i < 8; ++i) ok = ok & (a[i][0] == tlo) & ((a[i][1] & 0xffu) == thi) & (a[i][2] == tlo) & ((a[i][3] & 0xffu) == thi);
        if (__all(ok)) break;
      }
      STAMP(8);
      float v0 = 0.f, v1 = 0.f;
#pragma unroll
      for (int i = 0; i < 8; ++i) { v0 += __uint_as_float(a[i][1] & 0xffffff00u); v1 += __uint_as_float(a[i][3] & 0xffffff00u); }
      v0 = sum_b2to5(v0); v1 = sum_b2to5(v1);
      if (lane < 4) {
        *reinterpret_cast<unsigned*>(out + 8 * b + 2 * lane) = f2bf(v0) | (f2bf(v1) << 16);
        if (b == 0 && lane == 0) __hip_atomic_store(pE, E + 1, RLX_AGENT);
      }
      STAMP(9);
    }
    if (w == 0) { STAMP_FLUSH(0, 10); STAMP_FLUSH(14, 16); }
  } else {
    SYNC();
    const int kk = lane >> 4, c16 = lane & 15;
    const size_t key0 = (size_t)g * SK + (SK - WIN) + j * 128 + w * 16;
    u32x4 kf[4], vr[4];
#pragma unroll
    for (int st = 0; st < 4; ++st) kf[st] = ldg16(kc + (key0 + c16) * DH + 32 * st + 8 * kk);
#pragma unroll
    for (int i = 0; i < 4; ++i) vr[i] = ldg16(vc + (key0 + 4 * kk + i) * DH + 8 * c16);
    CBAR();
    STAMP(10);
#pragma unroll
    for (int st = 0; st < 4; ++st) asm volatile("" : "+v"(kf[st]));
    CBAR();
    SYNC();
    CBAR();
    u32x2 bfrag[8];
#pragma unroll
    for (int jj = 0; jj < 8; ++jj) {
#pragma unroll
      for (int d2 = 0; d2 < 2; ++d2) {
        const unsigned lo = vr[2 * d2][jj >> 1], hi = vr[2 * d2 + 1][jj >> 1];
        bfrag[jj][d2] = (jj & 1) ? __builtin_amdgcn_perm(hi, lo, 0x07060302u) : __builtin_amdgcn_perm(hi, lo, 0x05040100u);
      }
      asm volatile("" : "+v"(bfrag[jj]));
    }
    CBAR();
    STAMP(11);
    SYNC();
    STAMP(12);
    u32x4 qf[4];
#pragma unroll
    for (int st = 0; st < 4; ++st) qf[st] = *reinterpret_cast<const u32x4*>(&s_q[(c16 & 3) * 80 + 16 * st + 4 * kk]);
    f32x4 sacc = f32x4{0.f, 0.f, 0.f, 0.f};
#pragma unroll
    for (int st = 0; st < 4; ++st)
      sacc = __builtin_amdgcn_mfma_f32_16x16x32_bf16(__builtin_bit_cast(bf16x8, kf[st]), __builtin_bit_cast(bf16x8, qf[st]), sacc, 0, 0, 0);
    float sc[4];
#pragma unroll
    for (int i = 0; i < 4; ++i) sc[i] = rbf(rbf(sacc[i]) * SCALE);
    float m = fmaxf(fmaxf(sc[0], sc[1]), fmaxf(sc[2], sc[3]));
    m = x32_max(x16_max(m));
    float p[4];
#pragma unroll
    for (int i = 0; i < 4; ++i) p[i] = __builtin_amdgcn_exp2f((sc[i] - m) * LOG2E);
    float l = (p[0] + p[1]) + (p[2] + p[3]);
    l = x32_sum(x16_sum(l));
    u32x2 pf;
#pragma unroll
    for (int d2 = 0; d2 < 2; ++d2) pf[d2] = f2bf(p[2 * d2]) | (f2bf(p[2 * d2 + 1]) << 16);
    f32x4 oacc[8];
#pragma unroll
    for (int jj = 0; jj < 8; ++jj)
      oacc[jj] = __builtin_amdgcn_mfma_f32_16x16x16bf16_1k(__builtin_bit_cast(s16x4, pf), __builtin_bit_cast(s16x4, bfrag[jj]), f32x4{0.f, 0.f, 0.f, 0.f}, 0, 0, 0);
    if (lane < 16) {
#pragma unroll
      for (int v = 0; v < 4; ++v) {
        *reinterpret_cast<f32x4*>(&s_om[w][v][8 * lane]) = f32x4{oacc[0][v], oacc[1][v], oacc[2][v], oacc[3][v]};
        *reinterpret_cast<f32x4*>(&s_om[w][v][8 * lane + 4]) = f32x4{oacc[4][v], oacc[5][v], oacc[6][v], oacc[7][v]};
      }
      if (lane < 4) { s_mm[lane][w] = m; s_ll[lane][w] = l; }
    }
    STAMP(13);
    SYNC();
    if (w == 0) STAMP_FLUSH(10, 14);
  }
}

extern "C" void kernel_launch(void* const* d_in, const int* in_sizes, int n_in, void* d_out, int out_size, void* d_ws, size_t ws_size, hipStream_t stream) {
  fused_decode<<<256, 768, 0, stream>>>((const u16*)d_in[0], (const u16*)d_in[1], (const u16*)d_in[2], (const u16*)d_in[3], (const u16*)d_in[4],
                                        (u16*)d_out, (char*)d_ws);
}
```

```cpp
#include <hip/hip_runtime.h>
#include <hip/hip_bf16.h>
#include <stdint.h>

typedef unsigned short u16;
typedef unsigned long long u64;
typedef __attribute__((ext_vector_type(8))) __bf16 bf16x8;
typedef __attribute__((ext_vector_type(4))) float f32x4;
typedef __attribute__((ext_vector_type(4))) unsigned u32x4;
typedef __attribute__((ext_vector_type(2))) unsigned u32x2;
typedef __attribute__((ext_vector_type(4))) short s16x4;
#define GAS __attribute__((address_space(1)))
#define LAS __attribute__((address_space(3)))
typedef GAS u64 gu64;
typedef GAS unsigned gu32;
#define RLX_AGENT __ATOMIC_RELAXED, __HIP_MEMORY_SCOPE_AGENT

constexpr int D = 512, HQ = 32, HKV = 8, SK = 32768, DH = 128, WIN = 4096;
constexpr unsigned WS_E = 0;
constexpr unsigned WS_H2ML = 4096;
constexpr unsigned WS_H2O = 65536;
constexpr unsigned WS_H3 = WS_H2O + 524288;
constexpr unsigned WS_CNT = WS_H3 + 524288;
constexpr unsigned WS_H1 = WS_CNT + 3 * 256 * 256;
constexpr unsigned WS_END = WS_H1 + 131072;
constexpr float LOG2E = 1.4426950408889634f;
constexpr float SCALE = 0.08838834764831845f;
constexpr unsigned SPIN_CAP = 1u << 16;
constexpr unsigned HINT_CAP = 24;
#define TQ 300
#define KVGAP 4

__device__ __forceinline__ float bf2f(unsigned v) { return __uint_as_float(v << 16); }
__device__ __forceinline__ unsigned f2bf(float f) { __bf16 b = (__bf16)f; return (unsigned)__builtin_bit_cast(unsigned short, b); }
__device__ __forceinline__ float rbf(float f) { return bf2f(f2bf(f)); }
__device__ __forceinline__ u32x4 ldg16(const u16* p) { return __builtin_nontemporal_load(reinterpret_cast<const u32x4*>(p)); }
__device__ __forceinline__ float lo16(unsigned v) { return __uint_as_float(v << 16); }
__device__ __forceinline__ float hi16(unsigned v) { return __uint_as_float(v & 0xffff0000u); }
template <int CTRL> __device__ __forceinline__ float dpp(float x) {
  return __builtin_bit_cast(float, __builtin_amdgcn_mov_dpp(__builtin_bit_cast(int, x), CTRL, 0xf, 0xf, true));
}
constexpr int XOR1 = 0xB1, XOR2 = 0x4E, ROR4 = 0x124, ROR8 = 0x128;
__device__ __forceinline__ float x16_sum(float x) { auto s = __builtin_amdgcn_permlane16_swap(__float_as_uint(x), __float_as_uint(x), false, false); return __uint_as_float(s[0]) + __uint_as_float(s[1]); }
__device__ __forceinline__ float x32_sum(float x) { auto s = __builtin_amdgcn_permlane32_swap(__float_as_uint(x), __float_as_uint(x), false, false); return __uint_as_float(s[0]) + __uint_as_float(s[1]); }
__device__ __forceinline__ float x16_max(float x) { auto s = __builtin_amdgcn_permlane16_swap(__float_as_uint(x), __float_as_uint(x), false, false); return fmaxf(__uint_as_float(s[0]), __uint_as_float(s[1])); }
__device__ __forceinline__ float x32_max(float x) { auto s = __builtin_amdgcn_permlane32_swap(__float_as_uint(x), __float_as_uint(x), false, false); return fmaxf(__uint_as_float(s[0]), __uint_as_float(s[1])); }
__device__ __forceinline__ float sum_b1to5(float x) { x += dpp<XOR2>(x); x += dpp<ROR4>(x); x += dpp<ROR8>(x); x = x16_sum(x); return x32_sum(x); }
__device__ __forceinline__ float max_b1to5(float x) { x = fmaxf(x, dpp<XOR2>(x)); x = fmaxf(x, dpp<ROR4>(x)); x = fmaxf(x, dpp<ROR8>(x)); x = x16_max(x); return x32_max(x); }
__device__ __forceinline__ float sum_b2to5(float x) { x += dpp<ROR4>(x); x += dpp<ROR8>(x); x = x16_sum(x); return x32_sum(x); }

#define CBAR() do { asm volatile("" ::: "memory"); __builtin_amdgcn_sched_barrier(0); } while (0)
#define MEMBAR() asm volatile("" ::: "memory")
#define SYNC() do { __builtin_amdgcn_sched_barrier(0); __syncthreads(); __builtin_amdgcn_sched_barrier(0); } while (0)

__device__ __forceinline__ void hint_wait(gu32* c, unsigned n, int slp) {
#pragma unroll 1
  for (unsigned sp = 0; sp < HINT_CAP; ++sp) {
    MEMBAR();
    const unsigned v = __hip_atomic_load(c, RLX_AGENT);
    if (__builtin_amdgcn_readfirstlane(v) == n) break;
    if (slp) __builtin_amdgcn_s_sleep(2);
  }
}

__global__ __launch_bounds__(768, 1) void fused_decode(const u16* __restrict__ x, const u16* __restrict__ kc, const u16* __restrict__ vc,
                                                       const u16* __restrict__ wq, const u16* __restrict__ wo, u16* __restrict__ out, char* ws) {
  __shared__ __attribute__((aligned(16))) float s_qred[4][64];
  __shared__ __attribute__((aligned(16))) unsigned s_q[4 * 80];
  __shared__ __attribute__((aligned(16))) float s_om[8][4][128];
  __shared__ __attribute__((aligned(16))) float s_mm[4][8], s_ll[4][8];
  __shared__ __attribute__((aligned(16))) float s_cred[4][256];
  __shared__ __attribute__((aligned(16))) unsigned s_flag[4];

  const int b = blockIdx.x, g = b & 7, j = b >> 3;
  const int lane = threadIdx.x & 63, wv = __builtin_amdgcn_readfirstlane(threadIdx.x >> 6);
  const int w = wv < 4 ? wv : wv - 4, t = threadIdx.x & 255;
  const __amdgpu_buffer_rsrc_t rs = __builtin_amdgcn_make_buffer_rsrc(ws, 0, WS_END, 0x00020000);
  gu32* CNT = (gu32*)(ws + WS_CNT);

  asm volatile("" :: "s"(x), "s"(kc), "s"(vc), "s"(wq), "s"(wo), "s"(out), "s"(ws));
  const bool isA = wv < 4;
  const int dq = j >> 3, cb = j & 7;
  const int rbase = 128 * dq + (t >> 3);
  const int kk = lane >> 4, c16 = lane & 15;
  const size_t key0 = (size_t)g * SK + (SK - WIN) + j * 128 + w * 16;
  const GAS u32x4* p0 = isA ? (const GAS u32x4*)(wq + (size_t)rbase * 4096 + g * 512 + cb * 64 + 8 * (t & 7))
                            : (const GAS u32x4*)(kc + (key0 + c16) * DH + 8 * kk);
  const size_t st0 = isA ? (size_t)32 * 4096 / 8 : (size_t)4;
  asm volatile("" : "+v"(p0));
  if (!isA) SYNC();
  u32x4 ld0[4];
#pragma unroll
  for (int i = 0; i < 4; ++i) ld0[i] = __builtin_nontemporal_load(p0 + i * st0);
  CBAR();
  if (__builtin_expect(isA, 1)) {
    __builtin_amdgcn_s_setprio(3);
    const u64 tstart = __builtin_amdgcn_s_memrealtime();
    asm volatile("" :: "s"(wo), "s"(out));
    gu64* pE = (gu64*)(ws + WS_E);
    const u64 E = __hip_atomic_load(pE, RLX_AGENT);
    unsigned xv[4];
#pragma unroll
    for (int i = 0; i < 4; ++i) xv[i] = x[rbase + 32 * i];
    CBAR();
    gu64* myO = (gu64*)(ws + WS_H2O) + ((size_t)((4 * g + w) * 32 + j)) * 64 + lane;
    gu64* myML = (gu64*)(ws + WS_H2ML) + (4 * g + w) * 32 + j;
    __hip_atomic_store(myO, 0ull, RLX_AGENT);
    if (lane == 0) __hip_atomic_store(myML, 0ull, RLX_AGENT);
    if (t == 0) __hip_atomic_store(CNT + (256 + b) * 64, 0u, RLX_AGENT);
    if (t == 0) *(volatile LAS u32x4*)(&s_flag[0]) = u32x4{0u, 0u, 0u, 0u};
    CBAR();
    SYNC();
    u32x4* wqv = ld0;
    {
      float acc[8];
#pragma unroll
      for (int k = 0; k < 8; ++k) acc[k] = 0.f;
#pragma unroll
      for (int i = 0; i < 4; ++i) {
        const float xf = bf2f(xv[i]);
#pragma unroll
        for (int k = 0; k < 4; ++k) {
          acc[2 * k] = fmaf(xf, lo16(wqv[i][k]), acc[2 * k]);
          acc[2 * k + 1] = fmaf(xf, hi16(wqv[i][k]), acc[2 * k + 1]);
        }
      }
#pragma unroll
      for (int k = 0; k < 8; ++k) { float v = acc[k]; v += dpp<ROR8>(v); v = x16_sum(v); acc[k] = x32_sum(v); }
      if (lane < 8) {
        *reinterpret_cast<f32x4*>(&s_qred[w][8 * lane]) = f32x4{acc[0], acc[1], acc[2], acc[3]};
        *reinterpret_cast<f32x4*>(&s_qred[w][8 * lane + 4]) = f32x4{acc[4], acc[5], acc[6], acc[7]};
      }
    }
    const u64 tag40 = (E + 1) & 0xFFFFFFFFFFull;
    {
      const unsigned ftag = 1u;
      asm volatile("s_waitcnt vmcnt(0)" ::: "memory");
      if (lane == 0) *(volatile LAS unsigned*)(&s_flag[w]) = ftag;
      asm volatile("s_waitcnt lgkmcnt(0)" ::: "memory");
      const u32x4 fl = *(volatile LAS u32x4*)(&s_flag[0]);
      MEMBAR();
      if (fl[0] == ftag && fl[1] == ftag && fl[2] == ftag && fl[3] == ftag) {
        const float qv = (s_qred[0][lane] + s_qred[1][lane]) + (s_qred[2][lane] + s_qred[3][lane]);
        const unsigned fb = (__float_as_uint(qv) + 0x80u) >> 8;
        __hip_atomic_store((gu64*)(ws + WS_H1) + (g * 4 + dq) * 512 + cb * 64 + lane, ((u64)fb << 40) | tag40, RLX_AGENT);
      }
    }
    SYNC();
    while (__builtin_amdgcn_s_memrealtime() - tstart < TQ) __builtin_amdgcn_s_sleep(1);
    {
      const unsigned off = WS_H1 + (g * 2048 + 2 * t) * 8;
      const unsigned tlo = (unsigned)tag40, thi = (unsigned)(tag40 >> 32);
      u32x4 a[4];
#pragma unroll 1
      for (unsigned sp = 0; sp < SPIN_CAP; ++sp) {
        MEMBAR();
        bool ok = true;
#pragma unroll
        for (int i = 0; i < 4; ++i) a[i] = __builtin_amdgcn_raw_buffer_load_b128(rs, off + i * 4096, 0, 16);
#pragma unroll
        for (int i = 0; i < 4; ++i) ok = ok & (a[i][0] == tlo) & ((a[i][1] & 0xffu) == thi) & (a[i][2] == tlo) & ((a[i][3] & 0xffu) == thi);
        if (__all(ok)) break;
      }
      const float q0 = (__uint_as_float(a[0][1] & 0xffffff00u) + __uint_as_float(a[1][1] & 0xffffff00u)) + (__uint_as_float(a[2][1] & 0xffffff00u) + __uint_as_float(a[3][1] & 0xffffff00u));
      const float q1 = (__uint_as_float(a[0][3] & 0xffffff00u) + __uint_as_float(a[1][3] & 0xffffff00u)) + (__uint_as_float(a[2][3] & 0xffffff00u) + __uint_as_float(a[3][3] & 0xffffff00u));
      s_q[w * 80 + lane] = f2bf(q0) | (f2bf(q1) << 16);
    }
    SYNC();
    CBAR();
    const int hh = j >> 3, h = 4 * g + hh, kq = (j >> 1) & 3, ch = j & 1;
    const int r2 = lane >> 5, c32 = lane & 31;
    u32x4 wov[4];
#pragma unroll
    for (int i = 0; i < 4; ++i) wov[i] = ldg16(wo + (size_t)(h * 128 + 32 * kq + 8 * w + 2 * i + r2) * D + ch * 256 + 8 * c32);
    CBAR();
    SYNC();
    {
      float mw[8], lw[8];
      *reinterpret_cast<f32x4*>(&mw[0]) = *reinterpret_cast<const f32x4*>(&s_mm[w][0]);
      *reinterpret_cast<f32x4*>(&mw[4]) = *reinterpret_cast<const f32x4*>(&s_mm[w][4]);
      *reinterpret_cast<f32x4*>(&lw[0]) = *reinterpret_cast<const f32x4*>(&s_ll[w][0]);
      *reinterpret_cast<f32x4*>(&lw[4]) = *reinterpret_cast<const f32x4*>(&s_ll[w][4]);
      float ov0[8], ov1[8];
#pragma unroll
      for (int i = 0; i < 8; ++i) { const float2 o2 = *reinterpret_cast<const float2*>(&s_om[i][w][2 * lane]); ov0[i] = o2.x; ov1[i] = o2.y; }
      const float M = fmaxf(fmaxf(fmaxf(mw[0], mw[1]), fmaxf(mw[2], mw[3])), fmaxf(fmaxf(mw[4], mw[5]), fmaxf(mw[6], mw[7])));
      float L = 0.f, o0 = 0.f, o1 = 0.f;
#pragma unroll
      for (int i = 0; i < 8; ++i) {
        const float e = __builtin_amdgcn_exp2f((mw[i] - M) * LOG2E);
        L = fmaf(e, lw[i], L);
        o0 = fmaf(e, ov0[i], o0);
        o1 = fmaf(e, ov1[i], o1);
      }
      __hip_atomic_store(myO, (u64)__float_as_uint(o0) | ((u64)(__float_as_uint(o1) | 1u) << 32), RLX_AGENT);
      if (lane == 0) __hip_atomic_store(myML, (u64)__float_as_uint(M) | ((u64)(__float_as_uint(L) | 1u) << 32), RLX_AGENT);
      MEMBAR();
      if (lane < 8) __hip_atomic_fetch_add(CNT + (256 + (8 * w + lane) * 8 + g) * 64, 1u, RLX_AGENT);
    }
    float wf[4][8];
#pragma unroll
    for (int i = 0; i < 4; ++i)
#pragma unroll
      for (int k = 0; k < 4; ++k) { wf[i][2 * k] = lo16(wov[i][k]); wf[i][2 * k + 1] = hi16(wov[i][k]); }
#pragma unroll
    for (int i = 0; i < 4; ++i)
#pragma unroll
      for (int k = 0; k < 8; ++k) asm volatile("" : "+v"(wf[i][k]));
    CBAR();
    if (w == 0) hint_wait(CNT + (256 + b) * 64, 32u, 0);
    SYNC();
    {
      const int s = lane >> 1, q = lane & 1;
      const unsigned offO = WS_H2O + (unsigned)((h * 32 + s) * 64 + 16 * kq + 4 * w + 2 * q) * 8;
      gu64* srcML = (gu64*)(ws + WS_H2ML) + h * 32 + s;
      u32x4 a; u64 ml;
#pragma unroll 1
      for (unsigned sp = 0; sp < SPIN_CAP; ++sp) {
        MEMBAR();
        a = __builtin_amdgcn_raw_buffer_load_b128(rs, offO, 0, 16);
        ml = __hip_atomic_load(srcML, RLX_AGENT);
        const bool ok = (a[1] & a[3] & (unsigned)(ml >> 32) & 1u) != 0u;
        if (__all(ok)) break;
      }
      const float ms = __uint_as_float((unsigned)ml), ls = __uint_as_float((unsigned)(ml >> 32));
      const float M = max_b1to5(ms);
      const float wsx = __builtin_amdgcn_exp2f((ms - M) * LOG2E);
      float v5[5];
#pragma unroll
      for (int k = 0; k < 4; ++k) v5[k] = wsx * __uint_as_float(a[k]);
      v5[4] = wsx * ls;
#pragma unroll
      for (int k = 0; k < 5; ++k) v5[k] += dpp<XOR2>(v5[k]);
#pragma unroll
      for (int k = 0; k < 5; ++k) v5[k] += dpp<ROR4>(v5[k]);
#pragma unroll
      for (int k = 0; k < 5; ++k) v5[k] += dpp<ROR8>(v5[k]);
#pragma unroll
      for (int k = 0; k < 5; ++k) v5[k] = x16_sum(v5[k]);
#pragma unroll
      for (int k = 0; k < 5; ++k) v5[k] = x32_sum(v5[k]);
      const float inv = __builtin_amdgcn_rcpf(v5[4]);
      float own[4], lo[4], hi[4];
#pragma unroll
      for (int k = 0; k < 4; ++k) own[k] = rbf(v5[k] * inv);
#pragma unroll
      for (int k = 0; k < 4; ++k) { const float oth = dpp<XOR1>(own[k]); lo[k] = q ? oth : own[k]; hi[k] = q ? own[k] : oth; }
      float acc[8];
#pragma unroll
      for (int k = 0; k < 8; ++k) acc[k] = 0.f;
#pragma unroll
      for (int i = 0; i < 4; ++i) {
        const float of = (i < 2) ? (r2 ? lo[2 * (i & 1) + 1] : lo[2 * (i & 1)]) : (r2 ? hi[2 * (i & 1) + 1] : hi[2 * (i & 1)]);
#pragma unroll
        for (int k = 0; k < 8; ++k) acc[k] = fmaf(of, wf[i][k], acc[k]);
      }
      f32x4 r;
#pragma unroll
      for (int k = 0; k < 4; ++k) {
        auto sw = __builtin_amdgcn_permlane32_swap(__float_as_uint(acc[k]), __float_as_uint(acc[k + 4]), false, false);
        r[k] = __uint_as_float(sw[0]) + __uint_as_float(sw[1]);
      }
      *reinterpret_cast<f32x4*>(&s_cred[w][8 * c32 + 4 * r2]) = r;
    }
    SYNC();
    {
      const float v = (s_cred[0][t] + s_cred[1][t]) + (s_cred[2][t] + s_cred[3][t]);
      const unsigned fb = (__float_as_uint(v) + 0x80u) >> 8;
      __hip_atomic_store((gu64*)(ws + WS_H3) + (size_t)(h * 4 + kq) * 512 + ch * 256 + t, ((u64)fb << 40) | tag40, RLX_AGENT);
      MEMBAR();

    }
    if (w == 0 && b < 64) {

      __builtin_amdgcn_s_sleep(24);

      const int cp = lane & 3, rr = lane >> 2;
      const unsigned off = WS_H3 + (unsigned)(rr * 512 + 8 * b + 2 * cp) * 8;
      const unsigned tlo = (unsigned)tag40, thi = (unsigned)(tag40 >> 32);
      u32x4 a[8];
#pragma unroll 1
      for (unsigned sp = 0; sp < SPIN_CAP; ++sp) {
        MEMBAR();
        bool ok = true;
#pragma unroll
        for (int i = 0; i < 8; ++i) a[i] = __builtin_amdgcn_raw_buffer_load_b128(rs, off + i * 16 * 512 * 8, 0, 16);
#pragma unroll
        for (int i = 0; i < 8; ++i) ok = ok & (a[i][0] == tlo) & ((a[i][1] & 0xffu) == thi) & (a[i][2] == tlo) & ((a[i][3] & 0xffu) == thi);
        if (__all(ok)) break;
      }
      float v0 = 0.f, v1 = 0.f;
#pragma unroll
      for (int i = 0; i < 8; ++i) { v0 += __uint_as_float(a[i][1] & 0xffffff00u); v1 += __uint_as_float(a[i][3] & 0xffffff00u); }
      v0 = sum_b2to5(v0); v1 = sum_b2to5(v1);
      if (lane < 4) {
        *reinterpret_cast<unsigned*>(out + 8 * b + 2 * lane) = f2bf(v0) | (f2bf(v1) << 16);
        if (b == 0 && lane == 0) __hip_atomic_store(pE, E + 1, RLX_AGENT);
      }
    }
  } else {
    const GAS u32x4* vp = (const GAS u32x4*)(vc + (key0 + 4 * kk) * DH + 8 * c16);
    u32x4* kf = ld0;
    u32x4 vr[4];
    CBAR();
    __builtin_amdgcn_s_sleep(KVGAP);
    CBAR();
#pragma unroll
    for (int i = 0; i < 4; ++i) vr[i] = __builtin_nontemporal_load(vp + i * 16);
    CBAR();
#pragma unroll
    for (int st = 0; st < 4; ++st) asm volatile("" : "+v"(kf[st]));
    CBAR();
    SYNC();
    SYNC();
    u32x4 qf[4];
#pragma unroll
    for (int st = 0; st < 4; ++st) qf[st] = *reinterpret_cast<const u32x4*>(&s_q[(c16 & 3) * 80 + 16 * st + 4 * kk]);
    f32x4 sacc = f32x4{0.f, 0.f, 0.f, 0.f};
#pragma unroll
    for (int st = 0; st < 4; ++st)
      sacc = __builtin_amdgcn_mfma_f32_16x16x32_bf16(__builtin_bit_cast(bf16x8, kf[st]), __builtin_bit_cast(bf16x8, qf[st]), sacc, 0, 0, 0);
    float sc[4];
#pragma unroll
    for (int i = 0; i < 4; ++i) sc[i] = rbf(rbf(sacc[i]) * SCALE);
    float m = fmaxf(fmaxf(sc[0], sc[1]), fmaxf(sc[2], sc[3]));
    m = x32_max(x16_max(m));
    float p[4];
#pragma unroll
    for (int i = 0; i < 4; ++i) p[i] = __builtin_amdgcn_exp2f((sc[i] - m) * LOG2E);
    float l = (p[0] + p[1]) + (p[2] + p[3]);
    l = x32_sum(x16_sum(l));
    u32x2 pf;
#pragma unroll
    for (int d2 = 0; d2 < 2; ++d2) pf[d2] = f2bf(p[2 * d2]) | (f2bf(p[2 * d2 + 1]) << 16);
    asm volatile("" : "+v"(pf), "+v"(l), "+v"(m));
    CBAR();
    u32x2 bfrag[8];
#pragma unroll
    for (int jj = 0; jj < 8; ++jj)
#pragma unroll
      for (int d2 = 0; d2 < 2; ++d2) {
        const unsigned lo = vr[2 * d2][jj >> 1], hi = vr[2 * d2 + 1][jj >> 1];
        bfrag[jj][d2] = (jj & 1) ? __builtin_amdgcn_perm(hi, lo, 0x07060302u) : __builtin_amdgcn_perm(hi, lo, 0x05040100u);
      }
    f32x4 oacc[8];
#pragma unroll
    for (int jj = 0; jj < 8; ++jj)
      oacc[jj] = __builtin_amdgcn_mfma_f32_16x16x16bf16_1k(__builtin_bit_cast(s16x4, pf), __builtin_bit_cast(s16x4, bfrag[jj]), f32x4{0.f, 0.f, 0.f, 0.f}, 0, 0, 0);
    if (lane < 16) {
#pragma unroll
      for (int v = 0; v < 4; ++v) {
        *reinterpret_cast<f32x4*>(&s_om[w][v][8 * lane]) = f32x4{oacc[0][v], oacc[1][v], oacc[2][v], oacc[3][v]};
        *reinterpret_cast<f32x4*>(&s_om[w][v][8 * lane + 4]) = f32x4{oacc[4][v], oacc[5][v], oacc[6][v], oacc[7][v]};
      }
      if (lane < 4) { s_mm[lane][w] = m; s_ll[lane][w] = l; }
    }
    SYNC();
  }
}

extern "C" void kernel_launch(void* const* d_in, const int* in_sizes, int n_in, void* d_out, int out_size, void* d_ws, size_t ws_size, hipStream_t stream) {
  fused_decode<<<256, 768, 0, stream>>>((const u16*)d_in[0], (const u16*)d_in[1], (const u16*)d_in[2], (const u16*)d_in[3], (const u16*)d_in[4],
                                        (u16*)d_out, (char*)d_ws);
}
```
